```python
import torch

def _softmax(x, axis=-1):
    return torch.softmax(x, dim=axis)
def _gelu(x):
    return torch.nn.functional.gelu(x, approximate="tanh")
def _silu(x):
    return torch.nn.functional.silu(x)
def _sigmoid(x):
    return torch.sigmoid(x)
def _layernorm(x, axis=-1, eps=1e-5):
    m = x.mean(dim=axis, keepdim=True)
    v = x.var(dim=axis, keepdim=True, unbiased=False)
    return (x - m) / torch.sqrt(v + eps)
def _scatter_add(tgt, idx, src, axis=0):
    return tgt.clone().index_add_(axis, idx, src)
def _segment_sum(src, seg, n):
    out = torch.zeros((n,) + src.shape[1:], dtype=src.dtype)
    return out.index_add_(0, seg, src)
def _pad(x, pw):
    flat = tuple(v for pair in reversed(pw) for v in pair)
    return torch.nn.functional.pad(x, flat)

kernel_name = 'gated_mix__sliding_block__dense_activation_silu__prompt'

def setup_inputs():

    _w = (lambda a: (lambda t: t.to(torch.bfloat16) if t.is_floating_point() else t)(torch.as_tensor(a)))
    def _A_setup():
        B, S, D = 1, 2048, 576
        return {
            "x": _w((lambda *s: torch.randn(*s, dtype=torch.bfloat16) * 0.2)(B, S, D)),
            "wqkv": _w((lambda *s: torch.randn(*s, dtype=torch.bfloat16) * 0.2)(D, 3 * D)),
            "wo": _w((lambda *s: torch.randn(*s, dtype=torch.bfloat16) * 0.2)(D, D)),
        }
    _A_in = _A_setup()

    def _B_setup():
        B, S, D, F = 1, 2048, 576, 4608
        return {
            "x": _w((lambda *s: torch.randn(*s, dtype=torch.bfloat16) * 0.2)(B, S, D)),
            "w1": _w((lambda *s: torch.randn(*s, dtype=torch.bfloat16) * 3.2)(D, F) * (D ** -0.5)),
            "w2": _w((lambda *s: torch.randn(*s, dtype=torch.bfloat16) * 3.2)(F, D) * (F ** -0.5)),
        }
    _B_in = _B_setup()

    return {
        "A_x": _A_in["x"],
        "A_wqkv": _A_in["wqkv"],
        "A_wo": _A_in["wo"],
        "B_x": _B_in["x"],
        "B_w1": _B_in["w1"],
        "B_w2": _B_in["w2"],
        "gate_w": _w((lambda *s: torch.randn(*s, dtype=torch.bfloat16) * 0.2)(576) * 16.0),
    }

def reference(A_x, A_wqkv, A_wo, B_x, B_w1, B_w2, gate_w):
    def _A_ref(x, wqkv, wo):
        B, S, D = x.shape
        H = 32; Dh = D // H; W = 256
        qkv = (lambda x, *d: x.permute(*d))((x @ wqkv).reshape(B, S, 3, H, Dh), 2, 0, 3, 1, 4)
        q, k, v = qkv[0], qkv[1], qkv[2]
        scores = torch.einsum("bhqd,bhkd->bhqk", q, k) * (Dh ** -0.5)
        idx = torch.arange(S)
        mask = (idx[:, None] - idx[None, :] >= 0) & (idx[:, None] - idx[None, :] < W)
        scores = torch.where(mask[None, None], scores, -1e9)
        p = _softmax(scores, axis=-1)
        o = torch.einsum("bhqk,bhkd->bhqd", p, v)
        return (lambda x, *d: x.permute(*d))(o, 0, 2, 1, 3).reshape(B, S, D) @ wo

    def _B_ref(x, w1, w2):
        h = torch.einsum("bsd,df->bsf", x, w1)
        h = _silu(h)
        out = torch.einsum("bsf,fd->bsd", h, w2)
        return out

    x = A_x
    g = _sigmoid((x * gate_w).sum(-1, keepdims=True))
    a = _A_ref(x, A_wqkv, A_wo)
    b = _B_ref(x, B_w1, B_w2)
    return g * a + (1 - g) * b
```

```cpp
#include <hip/hip_runtime.h>
#include <hip/hip_bf16.h>
typedef __hip_bfloat16 bf16;
static __device__ __forceinline__ float b2f(bf16 v){ return __bfloat162float(v);}
static __device__ __forceinline__ float rb(float v){ return __bfloat162float(__float2bfloat16(v)); }
constexpr int S=2048,D=576,F=4608,H=32,DH=18,W=256;

template<int ACT>
__global__ void gemm(const bf16* A,const bf16* B,bf16* C,int M,int N,int K){
  __shared__ float As[16][65], Bs[16][65];
  int tx=threadIdx.x%16, ty=threadIdx.x/16;
  int m0=blockIdx.y*64, n0=blockIdx.x*64;
  float acc[4][4]={};
  for(int k0=0;k0<K;k0+=16){
    for(int i=threadIdx.x;i<64*16;i+=256){int r=i/16,c=i%16; As[c][r]=b2f(A[(size_t)(m0+r)*K+k0+c]);}
    for(int i=threadIdx.x;i<64*16;i+=256){int r=i/64,c=i%64; Bs[r][c]=b2f(B[(size_t)(k0+r)*N+n0+c]);}
    __syncthreads();
    for(int k=0;k<16;++k){
      float a[4],b[4];
      for(int i=0;i<4;++i){a[i]=As[k][ty*4+i]; b[i]=Bs[k][tx*4+i];}
      for(int i=0;i<4;++i)for(int j=0;j<4;++j)acc[i][j]+=a[i]*b[j];
    }
    __syncthreads();
  }
  for(int i=0;i<4;++i)for(int j=0;j<4;++j){
    float v=acc[i][j];
    if(ACT){ v=rb(v); v=v/(1.f+__expf(-v)); }
    C[(size_t)(m0+ty*4+i)*N+n0+tx*4+j]=__float2bfloat16(v);
  }
}
__global__ void attn(const bf16* qkv,bf16* o){
  int q=blockIdx.x*64+threadIdx.x, h=blockIdx.y;
  float qv[DH]; for(int d=0;d<DH;++d) qv[d]=b2f(qkv[(size_t)q*3*D+h*DH+d]);
  float m=-1e30f; int k0=q-W+1; if(k0<0)k0=0;
  const float sc=rsqrtf((float)DH);
  for(int k=k0;k<=q;++k){ float s=0; for(int d=0;d<DH;++d) s+=qv[d]*b2f(qkv[(size_t)k*3*D+D+h*DH+d]); m=fmaxf(m,s*sc);}
  float l=0,acc[DH]={};
  for(int k=k0;k<=q;++k){ float s=0; for(int d=0;d<DH;++d) s+=qv[d]*b2f(qkv[(size_t)k*3*D+D+h*DH+d]); float p=__expf(s*sc-m); l+=p; for(int d=0;d<DH;++d) acc[d]+=p*b2f(qkv[(size_t)k*3*D+2*D+h*DH+d]);}
  for(int d=0;d<DH;++d) o[(size_t)q*D+h*DH+d]=__float2bfloat16(acc[d]/l);
}
__global__ void fin(const bf16* x,const bf16* gw,const bf16* a,const bf16* b,bf16* out){
  int r=blockIdx.x; __shared__ float red[64];
  float s=0; for(int i=threadIdx.x;i<D;i+=64) s+=rb(b2f(x[(size_t)r*D+i])*b2f(gw[i]));
  for(int o=32;o;o>>=1) s+=__shfl_xor(s,o);
  s=rb(s); float g=rb(1.f/(1.f+__expf(-s))); float g1=rb(1.f-g);
  for(int i=threadIdx.x;i<D;i+=64) out[(size_t)r*D+i]=__float2bfloat16(rb(g*b2f(a[(size_t)r*D+i]))+rb(g1*b2f(b[(size_t)r*D+i])));
}
extern "C" void kernel_launch(void* const* d_in, const int* in_sizes, int n_in, void* d_out, int out_size, void* d_ws, size_t ws_size, hipStream_t stream) {
  const bf16* x=(const bf16*)d_in[0]; const bf16* wqkv=(const bf16*)d_in[1]; const bf16* wo=(const bf16*)d_in[2];
  const bf16* w1=(const bf16*)d_in[4]; const bf16* w2=(const bf16*)d_in[5]; const bf16* gw=(const bf16*)d_in[6];
  bf16* ws=(bf16*)d_ws; bf16* qkv=ws; bf16* h=qkv+(size_t)S*3*D; bf16* o=h+(size_t)S*F; bf16* a=o+(size_t)S*D; bf16* b=a+(size_t)S*D;
  gemm<0><<<dim3(3*D/64,S/64),256,0,stream>>>(x,wqkv,qkv,S,3*D,D);
  gemm<1><<<dim3(F/64,S/64),256,0,stream>>>(x,w1,h,S,F,D);
  attn<<<dim3(S/64,H),64,0,stream>>>(qkv,o);
  gemm<0><<<dim3(D/64,S/64),256,0,stream>>>(o,wo,a,S,D,D);
  gemm<0><<<dim3(D/64,S/64),256,0,stream>>>(h,w2,b,S,D,F);
  fin<<<S,64,0,stream>>>(x,gw,a,b,(bf16*)d_out);
}
```

```cpp
#include <hip/hip_runtime.h>
#include <hip/hip_bf16.h>
typedef __hip_bfloat16 bf16;
static __device__ __forceinline__ float b2f(bf16 v){ return __bfloat162float(v);}
static __device__ __forceinline__ float rb(float v){ return __bfloat162float(__float2bfloat16(v)); }
constexpr int S=2048,D=576,F=4608,H=32,DH=18,W=256;

template<int ACT>
__global__ void gemm(const bf16* A,const bf16* B,bf16* C,int M,int N,int K){
  __shared__ float As[16][65], Bs[16][65];
  int tx=threadIdx.x%16, ty=threadIdx.x/16;
  int m0=blockIdx.y*64, n0=blockIdx.x*64;
  float acc[4][4]={};
  for(int k0=0;k0<K;k0+=16){
    for(int i=threadIdx.x;i<64*16;i+=256){int r=i/16,c=i%16; As[c][r]=b2f(A[(size_t)(m0+r)*K+k0+c]);}
    for(int i=threadIdx.x;i<64*16;i+=256){int r=i/64,c=i%64; Bs[r][c]=b2f(B[(size_t)(k0+r)*N+n0+c]);}
    __syncthreads();
    for(int k=0;k<16;++k){
      float a[4],b[4];
      for(int i=0;i<4;++i){a[i]=As[k][ty*4+i]; b[i]=Bs[k][tx*4+i];}
      for(int i=0;i<4;++i)for(int j=0;j<4;++j)acc[i][j]+=a[i]*b[j];
    }
    __syncthreads();
  }
  for(int i=0;i<4;++i)for(int j=0;j<4;++j){
    float v=acc[i][j];
    if(ACT){ v=rb(v); v=v/(1.f+__expf(-v)); }
    C[(size_t)(m0+ty*4+i)*N+n0+tx*4+j]=__float2bfloat16(v);
  }
}
__global__ void attn(const bf16* qkv,bf16* o){
  int q=blockIdx.x*64+threadIdx.x, h=blockIdx.y;
  float qv[DH]; for(int d=0;d<DH;++d) qv[d]=b2f(qkv[(size_t)q*3*D+h*DH+d]);
  float m=-1e30f; int k0=q-W+1; if(k0<0)k0=0;
  const float sc=rsqrtf((float)DH);
  for(int k=k0;k<=q;++k){ float s=0; for(int d=0;d<DH;++d) s+=qv[d]*b2f(qkv[(size_t)k*3*D+D+h*DH+d]); m=fmaxf(m,s*sc);}
  float l=0,acc[DH]={};
  for(int k=k0;k<=q;++k){ float s=0; for(int d=0;d<DH;++d) s+=qv[d]*b2f(qkv[(size_t)k*3*D+D+h*DH+d]); float p=__expf(s*sc-m); l+=p; for(int d=0;d<DH;++d) acc[d]+=p*b2f(qkv[(size_t)k*3*D+2*D+h*DH+d]);}
  for(int d=0;d<DH;++d) o[(size_t)q*D+h*DH+d]=__float2bfloat16(acc[d]/l);
}
__global__ void fin(const bf16* x,const bf16* gw,const bf16* a,const bf16* b,bf16* out){
  int r=blockIdx.x; __shared__ float red[64];
  float s=0; for(int i=threadIdx.x;i<D;i+=64) s+=rb(b2f(x[(size_t)r*D+i])*b2f(gw[i]));
  for(int o=32;o;o>>=1) s+=__shfl_xor(s,o);
  s=rb(s); float g=rb(1.f/(1.f+__expf(-s))); float g1=rb(1.f-g);
  for(int i=threadIdx.x;i<D;i+=64) out[(size_t)r*D+i]=__float2bfloat16(rb(g*b2f(a[(size_t)r*D+i]))+rb(g1*b2f(b[(size_t)r*D+i])));
}

typedef __attribute__((address_space(3))) unsigned lds_u32;
typedef float f4 __attribute__((ext_vector_type(4)));
template<int MODE>
__global__ __launch_bounds__(512,2) void probe(const char* x,const char* w1,const char* w2,char* ws){
  extern __shared__ __attribute__((aligned(16))) char smem[];
  int tid=threadIdx.x,w=tid>>6,lane=tid&63; int b=blockIdx.x;
  unsigned long long r0=__builtin_amdgcn_s_memrealtime();
  if(MODE==0){
    const char* base=(b<128? w1+(size_t)b*40960 : w2+(size_t)(b-128)*40960)+lane*16;
    for(int i=0;i<5;++i) __builtin_amdgcn_global_load_lds((const __attribute__((address_space(1))) void*)(base+(size_t)(i*8+w)*1024),(lds_u32*)(smem+(w*16+i)*1024),16,0,0);
  } else if(MODE==1){
    if(w==0) __builtin_amdgcn_global_load_lds((const __attribute__((address_space(1))) void*)(x+(size_t)b*9216+lane*16),(lds_u32*)(smem),16,0,0);
  } else if(MODE==2){
    int xcd=b&7,slot=b>>3; int R=(xcd>>2)*8+(slot>>2);
    const char* base=x+(size_t)R*147456+lane*16;
    for(int i=0;i<18;++i){ __builtin_amdgcn_global_load_lds((const __attribute__((address_space(1))) void*)(base+(size_t)(i*8+w)*1024),(lds_u32*)(smem+(w*18+i)*1024),16,0,0); }
  } else if(MODE==3){
    f4 v={1.f*tid,2.f,3.f,4.f};
    char* base=ws+(size_t)(128<<20)+(size_t)b*65536+lane*16;
    for(int i=0;i<8;++i) asm volatile("global_store_dwordx4 %0, %1, off sc1\n s_nop 1"::"v"(base+(size_t)(i*8+w)*1024),"v"(v):"memory");
  }
  asm volatile("s_waitcnt vmcnt(0)":::"memory");
  __syncthreads();
  unsigned long long r1=__builtin_amdgcn_s_memrealtime();
  unsigned long long d=(r1-r0)*200;
  while(__builtin_amdgcn_s_memrealtime()-r0<d) __builtin_amdgcn_s_sleep(8);
}
extern "C" void kernel_launch(void* const* d_in, const int* in_sizes, int n_in, void* d_out, int out_size, void* d_ws, size_t ws_size, hipStream_t stream) {
  const bf16* x=(const bf16*)d_in[0]; const bf16* wqkv=(const bf16*)d_in[1]; const bf16* wo=(const bf16*)d_in[2];
  const bf16* w1=(const bf16*)d_in[4]; const bf16* w2=(const bf16*)d_in[5]; const bf16* gw=(const bf16*)d_in[6];
  bf16* ws=(bf16*)d_ws; bf16* qkv=ws; bf16* h=qkv+(size_t)S*3*D; bf16* o=h+(size_t)S*F; bf16* a=o+(size_t)S*D; bf16* b=a+(size_t)S*D;
  hipFuncSetAttribute((void*)probe<4>,hipFuncAttributeMaxDynamicSharedMemorySize,160*1024);
  probe<4><<<256,512,160*1024,stream>>>((const char*)x,(const char*)w1,(const char*)w2,(char*)d_ws);
  gemm<0><<<dim3(3*D/64,S/64),256,0,stream>>>(x,wqkv,qkv,S,3*D,D);
  gemm<1><<<dim3(F/64,S/64),256,0,stream>>>(x,w1,h,S,F,D);
  attn<<<dim3(S/64,H),64,0,stream>>>(qkv,o);
  gemm<0><<<dim3(D/64,S/64),256,0,stream>>>(o,wo,a,S,D,D);
  gemm<0><<<dim3(D/64,S/64),256,0,stream>>>(h,w2,b,S,D,F);
  fin<<<S,64,0,stream>>>(x,gw,a,b,(bf16*)d_out);
}
```

```cpp
#include <hip/hip_runtime.h>
#pragma clang diagnostic ignored "-Wint-to-pointer-cast"
typedef __bf16 b8 __attribute__((ext_vector_type(8)));
typedef __bf16 b2 __attribute__((ext_vector_type(2)));
typedef float f2 __attribute__((ext_vector_type(2)));
typedef float f4 __attribute__((ext_vector_type(4)));
typedef unsigned u4 __attribute__((ext_vector_type(4)));
typedef unsigned u2 __attribute__((ext_vector_type(2)));
typedef unsigned u6 __attribute__((ext_vector_type(6)));
typedef short s4 __attribute__((ext_vector_type(4)));
typedef short s8 __attribute__((ext_vector_type(8)));
typedef __amdgpu_buffer_rsrc_t rsrc_t;
#define LDSP __attribute__((address_space(3)))
#define DEV static __device__ __forceinline__
#define SB() __builtin_amdgcn_sched_barrier(0)
#define MF(c, a, b) c = __builtin_amdgcn_mfma_f32_16x16x32_bf16(a, b, c, 0, 0, 0); OPQ(c)
#define MA(c, a, b) asm volatile("v_mfma_f32_16x16x32_bf16 %0, %1, %2, %0" : "+v"(c) : "v"(a), "v"(b))
#define MZ(c, a, b) c = __builtin_amdgcn_mfma_f32_16x16x32_bf16(a, b, (f4){0.f, 0.f, 0.f, 0.f}, 0, 0, 0); OPQ(c)
#define MI(c, a, b, i) c = __builtin_amdgcn_mfma_f32_16x16x32_bf16(a, b, i, 0, 0, 0); OPQ(c)
#define MH(c, a, b) c = __builtin_amdgcn_mfma_f32_16x16x16bf16_1k(a, __builtin_bit_cast(s4, b), c, 0, 0, 0); OPQ(c)
#define TR(a, o) __builtin_amdgcn_ds_read_tr16_b64_v4i16((LDSP s4*)((a) + (o)))
#define LR(a, o) (*(LDSP u4*)((a) + (o)))
#define OPQ(x) asm volatile("" : "+v"(x))
#define RFL(x) __builtin_amdgcn_readfirstlane(x)
#define OOB 0x80000000u
#define FAR 0x7ffffff0u
#define LN(m) __builtin_amdgcn_inverse_ballot_w64(m)
#define OPS(x) asm volatile("" : "+s"(x))
DEV b8 cat(s4 a, s4 b) { return __builtin_bit_cast(b8, __builtin_shufflevector(a, b, 0, 1, 2, 3, 4, 5, 6, 7)); }
DEV b8 asb(u4 a) { return __builtin_bit_cast(b8, a); }
DEV unsigned pk(float a, float b) { b2 v = {(__bf16)a, (__bf16)b}; return __builtin_bit_cast(unsigned, v); }
DEV unsigned zz() { unsigned z = 0; OPQ(z); return z; }
DEV float u2f(unsigned a) { return __builtin_bit_cast(float, a); }
DEV unsigned f2u(float a) { return __builtin_bit_cast(unsigned, a); }
DEV float rb(float a) { return (float)(__bf16)a; }
DEV u2 pk4(f4 z) { return (u2){pk(z[0], z[1]), pk(z[2], z[3])}; }
DEV u4 cat2(u2 a, u2 b) { return (u4){a[0], a[1], b[0], b[1]}; }
DEV rsrc_t mk(const void* p, unsigned bytes) { return __builtin_amdgcn_make_buffer_rsrc((void*)p, 0, bytes, 0x00020000); }
DEV void st16(u4 v, rsrc_t r, unsigned vo, unsigned so) { __builtin_amdgcn_raw_buffer_store_b128(v, r, vo, so, 16); SB(); asm volatile("s_nop 1"); SB(); }
DEV void st8(u2 v, rsrc_t r, unsigned vo, unsigned so) { __builtin_amdgcn_raw_buffer_store_b64(v, r, vo, so, 16); }
DEV void st4(unsigned v, rsrc_t r, unsigned vo, unsigned so) { __builtin_amdgcn_raw_buffer_store_b32(v, r, vo, so, 16); }
DEV float mx3(float a, float b, float c) { return __builtin_fmaxf(__builtin_fmaxf(a, b), c); }
template <int IM> DEV void repoll(u2 v, rsrc_t ws, unsigned vo, unsigned so, unsigned long long tag) {
  unsigned n;
  asm volatile("s_mov_b32 %[n], 0x40000\nLP_%=:\n\tv_cmp_ne_u64 vcc, %[t], %[v]\n\ts_cbranch_vccz LQ_%=\n\ts_sub_u32 %[n], %[n], 1\n\ts_cbranch_scc1 LQ_%=\n\t"
               "s_sleep 1\n\tbuffer_load_dwordx2 %[v], %[o], %[r], %[s] offen offset:%[i] sc1\n\ts_waitcnt vmcnt(0) ; SLOW\n\ts_branch LP_%=\nLQ_%=:"
               : [v] "+v"(v), [n] "=&s"(n) : [o] "v"(vo), [r] "s"(ws), [t] "s"(tag), [s] "s"(so), [i] "n"(IM) : "memory", "vcc", "scc");
}

__global__ __launch_bounds__(512, 2) void gm_kernel(const char* __restrict__ px, const char* __restrict__ pqkv, const char* __restrict__ pwo,
    const char* __restrict__ pw1, const char* __restrict__ pw2, const char* __restrict__ pgw, char* pout, char* pws) {
  __shared__ __attribute__((aligned(16))) char smem[163840];
  const unsigned lb = (unsigned)(size_t)(LDSP char*)smem;
  const unsigned tid = threadIdx.x, lane = tid & 63, w = RFL(tid >> 6), b = blockIdx.x;
  const unsigned gi = b & 3, Rb = ((b >> 2) & 1) * 8 + (b >> 5), j = (b >> 3) & 3, c = 4 * gi + j, r = w & 3, cg = w >> 2;
  const unsigned n = lane & 15, g = lane >> 4;
  const unsigned wb = lb + w * 1024; unsigned v16 = lane * 16;
  const unsigned r8 = 8 * w + (lane >> 3), p8 = lane & 7;
  const unsigned vX = r8 * 1152 + ((p8 ^ ((r8 >> 1) & 7)) * 16);
  const unsigned vW1 = r8 * 9216 + ((p8 ^ ((((r8 >> 1) & 1) | ((w & 1) << 1)) << 1)) * 16);
  const unsigned o1h = (18 * gi + 9 * (j >> 1) + 4) * 128, o1 = (18 * gi + 9 * (j >> 1) + 5 * (j & 1)) * 128;
  asm volatile("" :: "s"(o1), "s"(o1h), "s"(576u * 9216u - o1), "s"(576u * 9216u - o1h), "s"(Rb * 147456u), "s"(wb), "v"(vX), "v"(vW1));
  const unsigned f1 = ((n >> 3) & 1) | ((g & 1) << 1), pWb = lb + (8 * g + (n >> 2)) * 128 + (n & 3) * 8;
  unsigned pW0 = pWb + (((2 * cg) ^ f1) * 32) + 98304u, pW1 = pW0 ^ 32, pWh = pWb + (((2 * (j & 1) + cg) ^ f1) * 32) + 65536u;
  OPQ(pW0); OPQ(pW1); OPQ(pWh);
  SB();
  asm volatile("" :: "s"(px), "s"(pqkv), "s"(pwo), "s"(pw1), "s"(pw2), "s"(pgw), "s"(pout), "s"(pws));
  const rsrc_t rX = mk(px + Rb * 147456u, 147456u);
  const rsrc_t rW1 = mk(pw1 + o1, 576u * 9216u - o1), rW1h = mk(pw1 + o1h, 576u * 9216u - o1h);
  SB(); asm volatile("s_nop 4"); SB();
  { unsigned t_; asm volatile("s_add_u32 m0, %[wb], 0\n\ts_mov_b32 %[t], 0\n\tbuffer_load_dwordx4 %[vX], %[rX], %[t] offen lds\n\ts_add_u32 m0, %[wb], 8192\n\ts_mov_b32 %[t], 73728\n\tbuffer_load_dwordx4 %[vX], %[rX], %[t] offen lds\n\ts_add_u32 m0, %[wb], 155648\n\ts_mov_b32 %[t], 0\n\tbuffer_load_dwordx4 %[vW1], %[rW1], %[t] offen lds\n\ts_add_u32 m0, %[wb], 139264\n\ts_mov_b32 %[t], 128\n\tbuffer_load_dwordx4 %[vW1], %[rW1], %[t] offen lds\n\ts_add_u32 m0, %[wb], 81920\n\ts_mov_b32 %[t], 0\n\tbuffer_load_dwordx4 %[vW1], %[rW1h], %[t] offen lds" : [t]"=&s"(t_) : [wb]"s"(wb), [vX]"v"(vX), [rX]"s"(rX), [vW1]"v"(vW1), [rW1]"s"(rW1), [rW1h]"s"(rW1h) : "memory", "scc"); }
  SB();
  unsigned cp0, cp1; { unsigned long long pc_; unsigned sz_; asm volatile("s_getpc_b64 %0\nLC0_:\n\ts_mov_b32 %1, LC1_-LC0_-4" : "=s"(pc_), "=s"(sz_));
    const unsigned a_ = ((b >> 3) * 8 + w) * 64, o2_ = a_ + 16384 < sz_ ? a_ + 16384 : sz_;
    asm volatile("s_load_dword %0, %2, %3\n\ts_load_dword %1, %2, %4" : "=&s"(cp0), "=&s"(cp1) : "s"(pc_), "s"(a_), "s"(o2_) : "memory"); } SB();
  const rsrc_t rWS = mk(pws, 67108864u);
  u2 ep; const unsigned vz = zz();
  { asm volatile("buffer_load_dwordx2 %[ep], %[vz], %[rWS], 0 offen sc1" : [ep]"=&v"(ep) : [vz]"v"(vz), [rWS]"s"(rWS) : "memory"); }
  SB();
  { unsigned t_; asm volatile("s_add_u32 m0, %[wb], 49152\n\ts_mov_b32 %[t], 128\n\tbuffer_load_dwordx4 %[vX], %[rX], %[t] offen lds\n\ts_add_u32 m0, %[wb], 57344\n\ts_mov_b32 %[t], 73856\n\tbuffer_load_dwordx4 %[vX], %[rX], %[t] offen lds\n\ts_add_u32 m0, %[wb], 122880\n\ts_mov_b32 %[t], 589824\n\tbuffer_load_dwordx4 %[vW1], %[rW1], %[t] offen lds\n\ts_add_u32 m0, %[wb], 98304\n\ts_mov_b32 %[t], 589952\n\tbuffer_load_dwordx4 %[vW1], %[rW1], %[t] offen lds" : [t]"=&s"(t_) : [wb]"s"(wb), [vX]"v"(vX), [rX]"s"(rX), [vW1]"v"(vW1), [rW1]"s"(rW1) : "memory", "scc"); }
  SB();
  const unsigned i8 = lane >> 3, c16 = p8 * 16, qid = 4 * Rb + j, l4 = lane & 4; unsigned vLS = __umul24(i8, 1152) + c16, vOB = OOB; OPQ(vLS); OPQ(vOB);
  u4 wq0, wq1; unsigned tc1, wqa, wqg, wq_o, wq_s, wq_l, wq_g, wq_t, wq_p;
  rsrc_t rT; { unsigned tv1_, y5; const bool mu = LN(0xccccccccccccccccull);
    if (!cg) { tv1_ = vLS + c16 + (c * 9216 + r * 256 + 128); rT = rX; OPQ(tv1_); } else { y5 = lane >> 5; tv1_ = __umul24(__umul24(y5, 60) + i8 + (64 + 128 * r + 4 * Rb), 9216) + (mu ? 512u >> (j & 1) : 2 * c16); rT = (j & 1) ? rW1h : rW1; OPQ(tv1_); }
  { asm volatile("s_nop 4\n\tbuffer_load_dword %[tc1], %[tv1_], %[rT], 0 offen" : [tc1]"=&v"(tc1) : [tv1_]"v"(tv1_), [rT]"s"(rT) : "memory"); }
  SB();
    const unsigned a_ = __umul24(i8, 3456) + __umul24(l4, 9), t_ = a_ | (v16 & 16), o_ = ((vW1 ^ ((w ^ j) << 6)) & 0x70) | (i8 << 7);
    if (!cg) {  const bool q3 = LN(0x8888888888888888ull);
      wq_s = mu ? vOB : t_; wq_l = q3 ? vOB : a_ + __umul24(lane & 3, 1152); wq_g = mu ? 2 * i8 : vOB; wq_o = o_ ^ (q3 ? 64 : 0);
      OPQ(wq_s); OPQ(wq_l); OPQ(wq_g); OPQ(wq_o); }
    else {  const unsigned e_ = mu ? 0 : 1152; wq_t = t_ + e_; OPQ(wq_t);
      if (w == 7) { const unsigned b_ = __umul24((lane >> 2) & 7, 36) + ((512 - 7 * qid) * 3456 - 216); const bool m_ = LN(0xccccccccull);
        wq_s = b_ + (v16 & 16) + (LN(0xffffffffull) ? 0 : e_ - 2304); OPQ(wq_s); wq_s = m_ ? vOB : wq_s; wq_l = LN(0x77777777ull) ? b_ + __umul24(lane & 3, 1152) : vOB;
        unsigned x_ = (qid & 2) * 16 + (qid & 8) * 8; OPS(x_); wq_g = m_ ? 1024 - 14 * qid : vOB;
        wq_o = __umul24(2 * (i8 & 3) + y5, 73728) + (c16 ^ x_ ^ (LN(0x88888888ull) ? 64 : 0)); OPQ(wq_s); OPQ(wq_l); OPQ(wq_g); OPQ(wq_o); } }
    wq_p = o_;
  }
  const rsrc_t rQKV = mk(pqkv, 576u * 3456u), rGW = mk(pgw, 1152u); const unsigned sqt = qid * 27648 + 72 * r + 288 * gi, sqg = 16 * qid;
  { asm volatile("s_bitcmp1_b32 0x8f, %[w]\n\ts_cbranch_scc0 LW_%=\n\tbuffer_load_dwordx4 %[wq0], %[wq_s], %[rQKV], %[sqt] offen offset:2304\n\tbuffer_load_dword %[wqa], %[wq_l], %[rQKV], %[sqt] offen offset:32\n\tbuffer_load_ushort %[wqg], %[wq_g], %[rGW], %[sqg] offen\n\tLW_%=:\n\ts_cmp_lt_u32 %[w], 4\n\ts_cbranch_scc1 LX_%=\n\tbuffer_load_dwordx4 %[wq1], %[wq_t], %[rQKV], %[sqt] offen\n\tLX_%=:" : [wq0]"=&v"(wq0), [wqa]"=&v"(wqa), [wqg]"=&v"(wqg), [wq1]"=&v"(wq1) : [w]"s"(w), [wq_s]"v"(wq_s), [rQKV]"s"(rQKV), [sqt]"s"(sqt), [wq_l]"v"(wq_l), [wq_g]"v"(wq_g), [rGW]"s"(rGW), [sqg]"s"(sqg), [wq_t]"v"(wq_t) : "memory", "scc"); }
  SB();
  unsigned lnY = 2 * j + (gi >> 1); asm volatile("" : "+s"(lnY)); unsigned lnX = lnY ^ 1; asm volatile("" : "+s"(lnX));
  OPQ(v16); unsigned vL = w * 1024 + v16; OPQ(vL);
  unsigned vF = v16 << 22; OPQ(vF);
  unsigned pX00 = lb + (32 * r + 16 * cg + n) * 128 + ((g ^ ((n >> 1) & 7)) * 16), pX10 = pX00 ^ 64, pX01 = pX00 ^ 2048, pX11 = pX00 ^ 2112;
  OPQ(pX00); OPQ(pX10); OPQ(pX01); OPQ(pX11);
  f4 cF000, cF001, cF010, cF011, cF100, cF101, cF110, cF111, cL000, cL001, cL010, cL100, cL101, cL110, cL111, cL200, cL201, cL210, cL211, cL300, cL301, cL310, cL311, cH0, cH1;
  unsigned vW2 = r8 * 1152 + ((p8 ^ (((r8 >> 1) & 3) << 1)) * 16); OPQ(vW2);
  unsigned pA0 = lb + cg * 4096 + (lane >> 2) * 128 + (lane & 3) * 8 + ((lane >> 3) & 3) * 32 + 57344u; OPQ(pA0);
  unsigned xa = (v16 + 3 * (v16 & 512)) ^ (lb + (8 + r) * 4096 + cg * 3072); OPQ(xa);
  unsigned vo_ = (4 * n + g) * 16; OPQ(vo_);
  SB();
  u4 pk0, pk1; unsigned po0 = (__umul24(l4 ^ ((~gi & 1) << 2), 20480) + (i8 << 6)) | ((v16 & 48) ^ (lane & 32)), ps1, po1; OPQ(po0);
  const unsigned hi_ = Rb >> 3, r7_ = 8 * (Rb & 7), g2_ = 1152 * gi + r7_;
  if (w == 2) { const unsigned mx_ = 0xffu << (8 * (lnX & 1)); unsigned a_ = 768 - 224 * j, b_ = Rb * 128 + (Rb & 6) * 16, c_ = 2048 + ((~gi & 1) << 10) + Rb * 64 + (Rb & 4) * 8;
    OPS(a_); OPS(b_); OPS(c_); const bool t_ = LN(0x30000ull); ps1 = t_ ? v16 + a_ : vOB; po1 = t_ ? c16 | (4096 + 32 * Rb) : vOB; unsigned y_ = ((l4 << 8) | (v16 & 48)) ^ c_; OPQ(ps1); OPQ(y_);
    ps1 = LN(0xffffull) ? v16 : ps1; po1 = LN(0xffffull) ? y_ : po1; po1 = LN(mx_) ? c16 ^ b_ : po1; ps1 += (144 * gi + 9 * (Rb & 14) + 16 + (Rb & 1)) * 1152 + 256 * j; po1 += 221184u; OPQ(ps1); OPQ(po1); }
  else { unsigned R_, Y_;
    if (r == 3) { R_ = g2_ + 256 + 576 * hi_; Y_ = 512 + 64 * hi_ + r7_; OPS(R_); OPS(Y_); }
    else if ((w >> 1) == 2) { R_ = 1152 * gi + 64 * (9 * (Rb & 1) + 5 * hi_ + (r7_ >> 4)) + 32 * r; Y_ = ((16 * r7_ + 64 * hi_) ^ (64 * j)) + 32 * (Rb & 4) + 32 * r; OPS(R_); OPS(Y_); }
    else { R_ = 144 * gi + 18 * (Rb & 4) + 36 * hi_ + (r7_ & 24) + (Rb & 2); Y_ = 1152 + 8 * Rb; OPS(R_); OPS(Y_); }
    if (cg) { const bool in_ = LN(w < 6 ? ~0ull : 0xffffull); ps1 = __umul24((lane >> 1) + R_, 1152) + ((v16 & 16) | (1024 + 32 * j)); po1 = v16 + 0u + 32 * Y_; OPQ(ps1); OPQ(po1);
      ps1 = in_ ? ps1 : vOB; po1 = in_ ? po1 : vOB; OPQ(ps1); OPQ(po1); }
    else { ps1 = __umul24(R_, 1152) + vLS + (w ? lnY : lnX) * 128; po1 = w ? po0 + 40960u + 64 * Y_ : (v16 ^ ((lane & 0x30) << 1)) + (204800u + 1024 * Rb); OPQ(ps1); OPQ(po1); } }
  const unsigned ss1 = 0, sd1 = 0;

  SB();
  { asm volatile("s_waitcnt vmcnt(7) lgkmcnt(0)\n\ts_barrier" :  :  : "memory"); }
  SB();
  const u4 x0_0 = LR(pX00, 0);
  const u4 x0_1 = LR(pX01, 0);
  s4 l1001 = TR(pW0, 57344);
  s4 h1001 = TR(pW0, 57856);
  s4 l1002 = TR(pW1, 57344);
  s4 h1002 = TR(pW1, 57856);
  s4 l1003 = TR(pW0, 40960);
  s4 h1003 = TR(pW0, 41472);
  SB();
  asm volatile("" :: "s"(cp0), "s"(cp1));
  SB();
  b8 f1001 = cat(l1001, h1001);
  MZ(cF000, f1001, asb(x0_0)); SB();
  s4 l1004 = TR(pW1, 40960);
  SB();
  { unsigned t_; asm volatile("s_add_u32 m0, %[wb], 65536\n\ts_mov_b32 %[t], 589824\n\tbuffer_load_dwordx4 %[vW1], %[rW1h], %[t] offen lds" : [t]"=&s"(t_) : [wb]"s"(wb), [vW1]"v"(vW1), [rW1h]"s"(rW1h) : "memory", "scc"); }
  SB();
  MZ(cF001, f1001, asb(x0_1)); SB();
  s4 h1004 = TR(pW1, 41472);
  SB();
  b8 f1002 = cat(l1002, h1002);
  MZ(cF010, f1002, asb(x0_0)); SB();
  s4 l1005 = TR(pWh, 16384);
  SB();
  { unsigned t_; asm volatile("s_add_u32 m0, %[wb], 32768\n\ts_mov_b32 %[t], 256\n\tbuffer_load_dwordx4 %[vX], %[rX], %[t] offen lds" : [t]"=&s"(t_) : [wb]"s"(wb), [vX]"v"(vX), [rX]"s"(rX) : "memory", "scc"); }
  SB();
  MZ(cF011, f1002, asb(x0_1)); SB();
  s4 h1005 = TR(pWh, 16896);
  SB();
  unsigned sFW = 4096u + gi * 1024, sGW = w == 0 ? sFW + (Rb * 4 + j) * 16 : FAR; OPS(sFW); OPS(sGW);
  SB();
  b8 f1003 = cat(l1003, h1003);
  MZ(cF100, f1003, asb(x0_0)); SB();
  const u4 x1_0 = LR(pX10, 0);
  const u4 x1_1 = LR(pX11, 0);
  SB();
  { unsigned t_; asm volatile("s_add_u32 m0, %[wb], 40960\n\ts_mov_b32 %[t], 73984\n\tbuffer_load_dwordx4 %[vX], %[rX], %[t] offen lds" : [t]"=&s"(t_) : [wb]"s"(wb), [vX]"v"(vX), [rX]"s"(rX) : "memory", "scc"); }
  SB();
  MZ(cF101, f1003, asb(x0_1)); SB();
  s4 l1006 = TR(pW0, 61440);
  s4 h1006 = TR(pW0, 61952);
  SB();
  unsigned ss0 = 9 * (w & 1) + 5 * hi_ + (w >> 1); OPS(ss0);
  SB();
  b8 f1004 = cat(l1004, h1004);
  MZ(cF110, f1004, asb(x0_0)); SB();
  s4 l1007 = TR(pW1, 61440);
  SB();
  { unsigned t_; asm volatile("s_add_u32 m0, %[wb], 106496\n\ts_mov_b32 %[t], 1179648\n\tbuffer_load_dwordx4 %[vW1], %[rW1], %[t] offen lds" : [t]"=&s"(t_) : [wb]"s"(wb), [vW1]"v"(vW1), [rW1]"s"(rW1) : "memory", "scc"); }
  SB();
  MZ(cF111, f1004, asb(x0_1)); SB();
  s4 h1007 = TR(pW1, 61952);
  SB();
  ss0 = (g2_ + 64 * ss0) * 1152 + lnY * 128; OPS(ss0);
  SB();
  b8 f1005 = cat(l1005, h1005);
  MZ(cH0, f1005, asb(x0_0)); SB();
  s4 l1008 = TR(pW0, 45056);
  SB();
  { unsigned t_; asm volatile("s_add_u32 m0, %[wb], 114688\n\ts_mov_b32 %[t], 1179776\n\tbuffer_load_dwordx4 %[vW1], %[rW1], %[t] offen lds" : [t]"=&s"(t_) : [wb]"s"(wb), [vW1]"v"(vW1), [rW1]"s"(rW1) : "memory", "scc"); }
  SB();
  MZ(cH1, f1005, asb(x0_1)); SB();
  s4 h1008 = TR(pW0, 45568);
  SB();
  const rsrc_t rW2 = mk(pw2, 4608u * 1152u); asm volatile("" :: "s"(rW2));
  SB();
  b8 f1006 = cat(l1006, h1006);
  MA(cF000, f1006, asb(x1_0)); SB();
  s4 l1009 = TR(pW1, 45056);
  SB();
  { unsigned t_; asm volatile("s_add_u32 m0, %[wb], 73728\n\ts_mov_b32 %[t], 1179648\n\tbuffer_load_dwordx4 %[vW1], %[rW1h], %[t] offen lds" : [t]"=&s"(t_) : [wb]"s"(wb), [vW1]"v"(vW1), [rW1h]"s"(rW1h) : "memory", "scc"); }
  SB();
  MA(cF001, f1006, asb(x1_1)); SB();
  s4 h1009 = TR(pW1, 45568);
  SB();
  unsigned st2 = (gi & 1) ? 0x40000000u : (Rb >> 1) * 589824 + 256 + (Rb & 1) * 128; OPS(st2);
  SB();
  b8 f1007 = cat(l1007, h1007);
  MA(cF010, f1007, asb(x1_0)); SB();
  s4 l1010 = TR(pWh, 20480);
  SB();
  { unsigned t_; asm volatile("s_add_u32 m0, %[wb], 16384\n\ts_mov_b32 %[t], 384\n\tbuffer_load_dwordx4 %[vX], %[rX], %[t] offen lds" : [t]"=&s"(t_) : [wb]"s"(wb), [vX]"v"(vX), [rX]"s"(rX) : "memory", "scc"); }
  SB();
  MA(cF011, f1007, asb(x1_1)); SB();
  s4 h1010 = TR(pWh, 20992);
  SB();
  unsigned sx2 = (512 - 28 * (Rb & 14) + w) * 9216; OPS(sx2);
  SB();
  { asm volatile("s_waitcnt vmcnt(6)\n\ts_barrier" :  :  : "memory"); }
  SB();
  SB();
  b8 f1008 = cat(l1008, h1008);
  MA(cF100, f1008, asb(x1_0)); SB();
  const u4 x2_0 = LR(pX00, 49152);
  const u4 x2_1 = LR(pX01, 49152);
  SB();
  { unsigned t_; asm volatile("s_add_u32 m0, %[wb], 24576\n\ts_mov_b32 %[t], 74112\n\tbuffer_load_dwordx4 %[vX], %[rX], %[t] offen lds" : [t]"=&s"(t_) : [wb]"s"(wb), [vX]"v"(vX), [rX]"s"(rX) : "memory", "scc"); }
  SB();
  MA(cF101, f1008, asb(x1_1)); SB();
  s4 l1011 = TR(pW0, 24576);
  s4 h1011 = TR(pW0, 25088);
  SB();
  const rsrc_t rWQ = mk(pws + 2097152u + c * 147456u, 147456u); asm volatile("" :: "s"(rWQ));
  SB();
  b8 f1009 = cat(l1009, h1009);
  MA(cF110, f1009, asb(x1_0)); SB();
  s4 l1012 = TR(pW1, 24576);
  SB();
  { unsigned t_; asm volatile("s_add_u32 m0, %[wb], 147456\n\ts_mov_b32 %[t], 1769472\n\tbuffer_load_dwordx4 %[vW1], %[rW1], %[t] offen lds" : [t]"=&s"(t_) : [wb]"s"(wb), [vW1]"v"(vW1), [rW1]"s"(rW1) : "memory", "scc"); }
  SB();
  MA(cF111, f1009, asb(x1_1)); SB();
  s4 h1012 = TR(pW1, 25088);
  SB();
  unsigned st3 = (gi & 1) ? 0x40000000u : (1152 * gi + 64 * Rb) * 1152 + lnX * 128; OPS(st3);
  SB();
  { asm volatile("s_waitcnt vmcnt(9)" : [wq0]"+v"(wq0), [wqa]"+v"(wqa), [wqg]"+v"(wqg), [wq1]"+v"(wq1), [ep]"+v"(ep) :  : "memory"); }
  const unsigned long long T_ = (((unsigned long long)(unsigned)RFL(ep[1]) << 32) | (unsigned)RFL(ep[0])) + 1; const unsigned tlo = (unsigned)T_, thi = (unsigned)(T_ >> 32);
  { const unsigned so = 2097152u + gi * 589824u;
    const u4 qa = {wq0[0] | __builtin_amdgcn_mov_dpp(wqa, 0x0f, 15, 15, true), wq0[1] | __builtin_amdgcn_mov_dpp(wqa, 0x5f, 15, 15, true), wq0[2] | wqg,
                   wq0[3] | __builtin_amdgcn_mov_dpp(wqa, 0xaf, 15, 15, true)};
    const unsigned sa_ = so + r * 147456 + qid * 1024, s1_ = sa_ + 73728, s0_ = cg ? so + 65536 + qid * 128 : sa_;
  { asm volatile("s_nop 1\n\ts_bitcmp1_b32 0x8f, %[w]\n\ts_cbranch_scc0 LW_%=\n\tbuffer_store_dwordx4 %[qa], %[wq_o], %[rWS], %[s0_] offen sc1\n\tLW_%=:\n\ts_cmp_lt_u32 %[w], 4\n\ts_cbranch_scc1 LX_%=\n\tbuffer_store_dwordx4 %[wq1], %[wq_p], %[rWS], %[s1_] offen sc1\n\tLX_%=:\n\ts_nop 2" :  : [w]"s"(w), [qa]"v"(qa), [wq_o]"v"(wq_o), [rWS]"s"(rWS), [s0_]"s"(s0_), [wq1]"v"(wq1), [wq_p]"v"(wq_p), [s1_]"s"(s1_) : "memory", "scc"); }
  }
  SB();
  b8 f1010 = cat(l1010, h1010);
  MA(cH0, f1010, asb(x1_0)); SB();
  s4 l1013 = TR(pW0, 0);
  SB();
  { unsigned t_; asm volatile("s_add_u32 m0, %[wb], 131072\n\ts_mov_b32 %[t], 1769600\n\tbuffer_load_dwordx4 %[vW1], %[rW1], %[t] offen lds" : [t]"=&s"(t_) : [wb]"s"(wb), [vW1]"v"(vW1), [rW1]"s"(rW1) : "memory", "scc"); }
  SB();
  MA(cH1, f1010, asb(x1_1)); SB();
  s4 h1013 = TR(pW0, 512);
  SB();
  unsigned sx3 = (1024 - 56 * Rb + w) * 1152; OPS(sx3);
  SB();
  b8 f1011 = cat(l1011, h1011);
  MA(cF000, f1011, asb(x2_0)); SB();
  s4 l1014 = TR(pW1, 0);
  SB();
  { unsigned t_; asm volatile("s_add_u32 m0, %[wb], 90112\n\ts_mov_b32 %[t], 1769472\n\tbuffer_load_dwordx4 %[vW1], %[rW1h], %[t] offen lds" : [t]"=&s"(t_) : [wb]"s"(wb), [vW1]"v"(vW1), [rW1h]"s"(rW1h) : "memory", "scc"); }
  SB();
  MA(cF001, f1011, asb(x2_1)); SB();
  s4 h1014 = TR(pW1, 512);
  SB();
  const rsrc_t rPK = mk(pws + 54525952u + c * 229376u, 225792u); asm volatile("" :: "s"(rPK));
  SB();
  b8 f1012 = cat(l1012, h1012);
  MA(cF010, f1012, asb(x2_0)); SB();
  s4 l1015 = TR(pWh, 0);
  SB();
  MA(cF011, f1012, asb(x2_1)); SB();
  s4 h1015 = TR(pWh, 512);
  SB();
  unsigned sd0 = ((2 * w + hi_) ^ j) + 2 * cg; OPS(sd0);
  SB();
  b8 f1013 = cat(l1013, h1013);
  MA(cF100, f1013, asb(x2_0)); SB();
  const u4 x3_0 = LR(pX10, 49152);
  const u4 x3_1 = LR(pX11, 49152);
  SB();
  { unsigned t_; asm volatile("s_add_u32 m0, %[wb], 0\n\ts_mov_b32 %[t], 512\n\tbuffer_load_dwordx4 %[vX], %[rX], %[t] offen lds" : [t]"=&s"(t_) : [wb]"s"(wb), [vX]"v"(vX), [rX]"s"(rX) : "memory", "scc"); }
  SB();
  MA(cF101, f1013, asb(x2_1)); SB();
  s4 l1016 = TR(pW0, 28672);
  s4 h1016 = TR(pW0, 29184);
  SB();
  sd0 = 40960u + (64 * sd0 + r7_) * 64; OPS(sd0);
  SB();
  b8 f1014 = cat(l1014, h1014);
  MA(cF110, f1014, asb(x2_0)); SB();
  s4 l1017 = TR(pW1, 28672);
  SB();
  const unsigned sH = 33554432u + (Rb * 4 + gi) * 18 * 16384 + r * 2048, mF = 4 * j; asm volatile("" :: "s"(sH), "s"(mF));
  SB();
  MA(cF111, f1014, asb(x2_1)); SB();
  s4 h1017 = TR(pW1, 29184);
  SB();
  unsigned sGX = (w) == 0 ? sFW + 4096 + (j * 16 + Rb) * 16 : FAR; OPS(sGX);
  SB();
  b8 f1015 = cat(l1015, h1015);
  MA(cH0, f1015, asb(x2_0)); SB();
  s4 l1018 = TR(pW0, 4096);
  SB();
  { unsigned t_; asm volatile("s_add_u32 m0, %[wb], 8192\n\ts_mov_b32 %[t], 74240\n\tbuffer_load_dwordx4 %[vX], %[rX], %[t] offen lds" : [t]"=&s"(t_) : [wb]"s"(wb), [vX]"v"(vX), [rX]"s"(rX) : "memory", "scc"); }
  SB();
  MA(cH1, f1015, asb(x2_1)); SB();
  s4 h1018 = TR(pW0, 4608);
  SB();
  unsigned sGK = 65536u + (c * 8 + w) * 512 + Rb * 16; OPS(sGK);
  SB();
  b8 f1016 = cat(l1016, h1016);
  MA(cF000, f1016, asb(x3_0)); SB();
  s4 l1019 = TR(pW1, 4096);
  SB();
  unsigned sFK = sGK - (Rb >= 2 ? 32 : Rb * 16); OPS(sFK);
  SB();
  MA(cF001, f1016, asb(x3_1)); SB();
  s4 h1019 = TR(pW1, 4608);
  SB();
  unsigned sFH = 131072u + ((Rb * 4 + gi) * 4 + r) * 512; OPS(sFH);
  SB();
  b8 f1017 = cat(l1017, h1017);
  MA(cF010, f1017, asb(x3_0)); SB();
  s4 l1020 = TR(pWh, 4096);
  SB();
  { unsigned t_; asm volatile("s_add_u32 m0, %[wb], 155648\n\ts_mov_b32 %[t], 2359296\n\tbuffer_load_dwordx4 %[vW1], %[rW1], %[t] offen lds" : [t]"=&s"(t_) : [wb]"s"(wb), [vW1]"v"(vW1), [rW1]"s"(rW1) : "memory", "scc"); }
  SB();
  MA(cF011, f1017, asb(x3_1)); SB();
  s4 h1020 = TR(pWh, 4608);
  SB();
  unsigned sGH = sFH + (2 * j + cg) * 16; OPS(sGH);
  SB();
  { asm volatile("s_waitcnt vmcnt(9)\n\ts_barrier" :  :  : "memory"); }
  SB();
  b8 f1018 = cat(l1018, h1018);
  MA(cF100, f1018, asb(x3_0)); SB();
  const u4 x4_0 = LR(pX00, 32768);
  const u4 x4_1 = LR(pX01, 32768);
  SB();
  unsigned sFP = 262144u + ((Rb * 4 + j) * 4 + r) * 256; OPS(sFP);
  SB();
  MA(cF101, f1018, asb(x3_1)); SB();
  s4 l1021 = TR(pW0, 8192);
  s4 h1021 = TR(pW0, 8704);
  SB();
  b8 f1019 = cat(l1019, h1019);
  MA(cF110, f1019, asb(x3_0)); SB();
  s4 l1022 = TR(pW1, 8192);
  SB();
  { unsigned t_; asm volatile("s_add_u32 m0, %[wb], 122880\n\ts_mov_b32 %[t], 2359424\n\tbuffer_load_dwordx4 %[vW1], %[rW1], %[t] offen lds" : [t]"=&s"(t_) : [wb]"s"(wb), [vW1]"v"(vW1), [rW1]"s"(rW1) : "memory", "scc"); }
  SB();
  MA(cF111, f1019, asb(x3_1)); SB();
  s4 h1022 = TR(pW1, 8704);
  SB();
  b8 f1020 = cat(l1020, h1020);
  MA(cH0, f1020, asb(x3_0)); SB();
  s4 l1023 = TR(pW0, 16384);
  SB();
  MA(cH1, f1020, asb(x3_1)); SB();
  s4 h1023 = TR(pW0, 16896);
  SB();
  b8 f1021 = cat(l1021, h1021);
  MA(cF000, f1021, asb(x4_0)); SB();
  s4 l1024 = TR(pW1, 16384);
  SB();
  { unsigned t_; asm volatile("s_add_u32 m0, %[wb], 81920\n\ts_mov_b32 %[t], 2359296\n\tbuffer_load_dwordx4 %[vW1], %[rW1h], %[t] offen lds" : [t]"=&s"(t_) : [wb]"s"(wb), [vW1]"v"(vW1), [rW1h]"s"(rW1h) : "memory", "scc"); }
  SB();
  MA(cF001, f1021, asb(x4_1)); SB();
  s4 h1024 = TR(pW1, 16896);
  SB();
  b8 f1022 = cat(l1022, h1022);
  MA(cF010, f1022, asb(x4_0)); SB();
  s4 l1025 = TR(pWh, 8192);
  SB();
  MA(cF011, f1022, asb(x4_1)); SB();
  s4 h1025 = TR(pWh, 8704);
  SB();
  b8 f1023 = cat(l1023, h1023);
  MA(cF100, f1023, asb(x4_0)); SB();
  const u4 x5_0 = LR(pX10, 32768);
  const u4 x5_1 = LR(pX11, 32768);
  SB();
  unsigned tc2 = __umul24(v16 & 16, 144) + vW1; asm volatile("v_writelane_b32 %0, %1, 62\n\tv_writelane_b32 %0, %2, 63" : "+v"(tc2) : "s"(sx2), "s"(sx2 + 2304));
  { asm volatile("buffer_load_dword %[tc2], %[tc2], %[rW1], %[st2] offen" : [tc2]"+v"(tc2) : [rW1]"s"(rW1), [st2]"s"(st2) : "memory"); }
  SB();
  MA(cF101, f1023, asb(x4_1)); SB();
  s4 l1026 = TR(pW0, 12288);
  s4 h1026 = TR(pW0, 12800);
  SB();
  b8 f1024 = cat(l1024, h1024);
  MA(cF110, f1024, asb(x4_0)); SB();
  s4 l1027 = TR(pW1, 12288);
  SB();
  MA(cF111, f1024, asb(x4_1)); SB();
  s4 h1027 = TR(pW1, 12800);
  SB();
  b8 f1025 = cat(l1025, h1025);
  MA(cH0, f1025, asb(x4_0)); SB();
  s4 l1028 = TR(pW0, 20480);
  SB();
  { unsigned t_; asm volatile("s_add_u32 m0, %[wb], 49152\n\ts_mov_b32 %[t], 640\n\tbuffer_load_dwordx4 %[vX], %[rX], %[t] offen lds" : [t]"=&s"(t_) : [wb]"s"(wb), [vX]"v"(vX), [rX]"s"(rX) : "memory", "scc"); }
  SB();
  MA(cH1, f1025, asb(x4_1)); SB();
  s4 h1028 = TR(pW0, 20992);
  SB();
  b8 f1026 = cat(l1026, h1026);
  MA(cF000, f1026, asb(x5_0)); SB();
  s4 l1029 = TR(pW1, 20480);
  SB();
  MA(cF001, f1026, asb(x5_1)); SB();
  s4 h1029 = TR(pW1, 20992);
  SB();
  b8 f1027 = cat(l1027, h1027);
  MA(cF010, f1027, asb(x5_0)); SB();
  s4 l1030 = TR(pWh, 12288);
  SB();
  { unsigned t_; asm volatile("s_add_u32 m0, %[wb], 57344\n\ts_mov_b32 %[t], 74368\n\tbuffer_load_dwordx4 %[vX], %[rX], %[t] offen lds" : [t]"=&s"(t_) : [wb]"s"(wb), [vX]"v"(vX), [rX]"s"(rX) : "memory", "scc"); }
  SB();
  MA(cF011, f1027, asb(x5_1)); SB();
  s4 h1030 = TR(pWh, 12800);
  SB();
  { asm volatile("s_waitcnt vmcnt(8)\n\ts_barrier" :  :  : "memory"); }
  SB();
  st8((u2){tlo, thi}, rWS, vF + 0, sGW); SB();
  SB();
  { asm volatile("s_waitcnt vmcnt(22)" : [tc1]"+v"(tc1) :  : "memory"); }
  SB();
  b8 f1028 = cat(l1028, h1028);
  MA(cF100, f1028, asb(x5_0)); SB();
  const u4 x6_0 = LR(pX00, 16384);
  const u4 x6_1 = LR(pX01, 16384);
  SB();
  MA(cF101, f1028, asb(x5_1)); SB();
  s4 l1031 = TR(pW0, 49152);
  s4 h1031 = TR(pW0, 49664);
  SB();
  b8 f1029 = cat(l1029, h1029);
  MA(cF110, f1029, asb(x5_0)); SB();
  s4 l1032 = TR(pW1, 49152);
  SB();
  { unsigned t_; asm volatile("s_add_u32 m0, %[wb], 139264\n\ts_mov_b32 %[t], 2949120\n\tbuffer_load_dwordx4 %[vW1], %[rW1], %[t] offen lds" : [t]"=&s"(t_) : [wb]"s"(wb), [vW1]"v"(vW1), [rW1]"s"(rW1) : "memory", "scc"); }
  SB();
  MA(cF111, f1029, asb(x5_1)); SB();
  s4 h1032 = TR(pW1, 49664);
  SB();
  b8 f1030 = cat(l1030, h1030);
  MA(cH0, f1030, asb(x5_0)); SB();
  s4 l1033 = TR(pW0, 32768);
  SB();
  MA(cH1, f1030, asb(x5_1)); SB();
  s4 h1033 = TR(pW0, 33280);
  SB();
  SB();
  b8 f1031 = cat(l1031, h1031);
  MA(cF000, f1031, asb(x6_0)); SB();
  s4 l1034 = TR(pW1, 32768);
  SB();
  { unsigned t_; asm volatile("s_add_u32 m0, %[wb], 106496\n\ts_mov_b32 %[t], 2949248\n\tbuffer_load_dwordx4 %[vW1], %[rW1], %[t] offen lds" : [t]"=&s"(t_) : [wb]"s"(wb), [vW1]"v"(vW1), [rW1]"s"(rW1) : "memory", "scc"); }
  SB();
  MA(cF001, f1031, asb(x6_1)); SB();
  s4 h1034 = TR(pW1, 33280);
  SB();
  b8 f1032 = cat(l1032, h1032);
  MA(cF010, f1032, asb(x6_0)); SB();
  s4 l1035 = TR(pWh, 24576);
  SB();
  MA(cF011, f1032, asb(x6_1)); SB();
  s4 h1035 = TR(pWh, 25088);
  SB();
  b8 f1033 = cat(l1033, h1033);
  MA(cF100, f1033, asb(x6_0)); SB();
  const u4 x7_0 = LR(pX10, 16384);
  const u4 x7_1 = LR(pX11, 16384);
  SB();
  { unsigned t_; asm volatile("s_add_u32 m0, %[wb], 65536\n\ts_mov_b32 %[t], 2949120\n\tbuffer_load_dwordx4 %[vW1], %[rW1h], %[t] offen lds" : [t]"=&s"(t_) : [wb]"s"(wb), [vW1]"v"(vW1), [rW1h]"s"(rW1h) : "memory", "scc"); }
  SB();
  MA(cF101, f1033, asb(x6_1)); SB();
  s4 l1036 = TR(pW0, 53248);
  s4 h1036 = TR(pW0, 53760);
  SB();
  b8 f1034 = cat(l1034, h1034);
  MA(cF110, f1034, asb(x6_0)); SB();
  s4 l1037 = TR(pW1, 53248);
  SB();
  MA(cF111, f1034, asb(x6_1)); SB();
  s4 h1037 = TR(pW1, 53760);
  SB();
  b8 f1035 = cat(l1035, h1035);
  MA(cH0, f1035, asb(x6_0)); SB();
  s4 l1038 = TR(pW0, 36864);
  SB();
  { unsigned t_; asm volatile("s_add_u32 m0, %[wb], 32768\n\ts_mov_b32 %[t], 768\n\tbuffer_load_dwordx4 %[vX], %[rX], %[t] offen lds" : [t]"=&s"(t_) : [wb]"s"(wb), [vX]"v"(vX), [rX]"s"(rX) : "memory", "scc"); }
  SB();
  MA(cH1, f1035, asb(x6_1)); SB();
  s4 h1038 = TR(pW0, 37376);
  SB();
  b8 f1036 = cat(l1036, h1036);
  MA(cF000, f1036, asb(x7_0)); SB();
  s4 l1039 = TR(pW1, 36864);
  SB();
  MA(cF001, f1036, asb(x7_1)); SB();
  s4 h1039 = TR(pW1, 37376);
  SB();
  b8 f1037 = cat(l1037, h1037);
  MA(cF010, f1037, asb(x7_0)); SB();
  s4 l1040 = TR(pWh, 28672);
  SB();
  { unsigned t_; asm volatile("s_add_u32 m0, %[wb], 40960\n\ts_mov_b32 %[t], 74496\n\tbuffer_load_dwordx4 %[vX], %[rX], %[t] offen lds" : [t]"=&s"(t_) : [wb]"s"(wb), [vX]"v"(vX), [rX]"s"(rX) : "memory", "scc"); }
  SB();
  MA(cF011, f1037, asb(x7_1)); SB();
  s4 h1040 = TR(pWh, 29184);
  SB();
  { asm volatile("s_waitcnt vmcnt(9)\n\ts_barrier" :  :  : "memory"); }
  SB();
  b8 f1038 = cat(l1038, h1038);
  MA(cF100, f1038, asb(x7_0)); SB();
  const u4 x8_0 = LR(pX00, 0);
  const u4 x8_1 = LR(pX01, 0);
  SB();
  MA(cF101, f1038, asb(x7_1)); SB();
  s4 l1041 = TR(pW0, 57344);
  s4 h1041 = TR(pW0, 57856);
  SB();
  b8 f1039 = cat(l1039, h1039);
  MA(cF110, f1039, asb(x7_0)); SB();
  s4 l1042 = TR(pW1, 57344);
  SB();
  const bool wo_ = (0x47 >> w) & 1; const rsrc_t rPS = mk(wo_ ? pwo : pw2, wo_ ? 576u * 1152u : 4608u * 1152u);
  { asm volatile("s_nop 2\n\tbuffer_load_dwordx4 %[pk1], %[ps1], %[rPS], 0 offen" : [pk1]"=&v"(pk1) : [ps1]"v"(ps1), [rPS]"s"(rPS) : "memory"); }
  SB();
  MA(cF111, f1039, asb(x7_1)); SB();
  s4 h1042 = TR(pW1, 57856);
  SB();
  b8 f1040 = cat(l1040, h1040);
  MA(cH0, f1040, asb(x7_0)); SB();
  s4 l1043 = TR(pW0, 24576);
  SB();
  MA(cH1, f1040, asb(x7_1)); SB();
  s4 h1043 = TR(pW0, 25088);
  SB();
  b8 f1041 = cat(l1041, h1041);
  MA(cF000, f1041, asb(x8_0)); SB();
  s4 l1044 = TR(pW1, 24576);
  SB();
  { asm volatile("s_nop 2\n\tbuffer_load_dwordx4 %[pk0], %[vLS], %[rW2], %[ss0] offen" : [pk0]"=&v"(pk0) : [vLS]"v"(vLS), [rW2]"s"(rW2), [ss0]"s"(ss0) : "memory"); }
  SB();
  MA(cF001, f1041, asb(x8_1)); SB();
  s4 h1044 = TR(pW1, 25088);
  SB();
  b8 f1042 = cat(l1042, h1042);
  MA(cF010, f1042, asb(x8_0)); SB();
  s4 l1045 = TR(pWh, 16384);
  SB();
  MA(cF011, f1042, asb(x8_1)); SB();
  s4 h1045 = TR(pWh, 16896);
  SB();
  b8 f1043 = cat(l1043, h1043);
  MA(cF100, f1043, asb(x8_0)); SB();
  const u4 x9_0 = LR(pX10, 0);
  const u4 x9_1 = LR(pX11, 0);
  SB();
  { unsigned t_; asm volatile("s_add_u32 m0, %[wb], 98304\n\ts_mov_b32 %[t], 3538944\n\tbuffer_load_dwordx4 %[vW1], %[rW1], %[t] offen lds" : [t]"=&s"(t_) : [wb]"s"(wb), [vW1]"v"(vW1), [rW1]"s"(rW1) : "memory", "scc"); }
  SB();
  MA(cF101, f1043, asb(x8_1)); SB();
  s4 l1046 = TR(pW0, 61440);
  s4 h1046 = TR(pW0, 61952);
  SB();
  b8 f1044 = cat(l1044, h1044);
  MA(cF110, f1044, asb(x8_0)); SB();
  s4 l1047 = TR(pW1, 61440);
  SB();
  MA(cF111, f1044, asb(x8_1)); SB();
  s4 h1047 = TR(pW1, 61952);
  SB();
  b8 f1045 = cat(l1045, h1045);
  MA(cH0, f1045, asb(x8_0)); SB();
  s4 l1048 = TR(pW0, 28672);
  SB();
  { unsigned t_; asm volatile("s_add_u32 m0, %[wb], 147456\n\ts_mov_b32 %[t], 3539072\n\tbuffer_load_dwordx4 %[vW1], %[rW1], %[t] offen lds" : [t]"=&s"(t_) : [wb]"s"(wb), [vW1]"v"(vW1), [rW1]"s"(rW1) : "memory", "scc"); }
  SB();
  MA(cH1, f1045, asb(x8_1)); SB();
  s4 h1048 = TR(pW0, 29184);
  SB();
  b8 f1046 = cat(l1046, h1046);
  MA(cF000, f1046, asb(x9_0)); SB();
  s4 l1049 = TR(pW1, 28672);
  SB();
  MA(cF001, f1046, asb(x9_1)); SB();
  s4 h1049 = TR(pW1, 29184);
  SB();
  b8 f1047 = cat(l1047, h1047);
  MA(cF010, f1047, asb(x9_0)); SB();
  s4 l1050 = TR(pWh, 20480);
  SB();
  { unsigned t_; asm volatile("s_add_u32 m0, %[wb], 73728\n\ts_mov_b32 %[t], 3538944\n\tbuffer_load_dwordx4 %[vW1], %[rW1h], %[t] offen lds" : [t]"=&s"(t_) : [wb]"s"(wb), [vW1]"v"(vW1), [rW1h]"s"(rW1h) : "memory", "scc"); }
  SB();
  MA(cF011, f1047, asb(x9_1)); SB();
  s4 h1050 = TR(pWh, 20992);
  SB();
  { asm volatile("s_waitcnt vmcnt(7)\n\ts_barrier" :  :  : "memory"); }
  SB();
  SB();
  b8 f1048 = cat(l1048, h1048);
  MA(cF100, f1048, asb(x9_0)); SB();
  const u4 x10_0 = LR(pX00, 49152);
  const u4 x10_1 = LR(pX01, 49152);
  SB();
  MA(cF101, f1048, asb(x9_1)); SB();
  s4 l1051 = TR(pW0, 40960);
  s4 h1051 = TR(pW0, 41472);
  SB();
  u2 pFW;
  { asm volatile("buffer_load_dwordx2 %[pFW], %[v16], %[rWS], %[sFW] offen offset:0 sc1" : [pFW]"=&v"(pFW) : [v16]"v"(v16), [rWS]"s"(rWS), [sFW]"s"(sFW) : "memory"); }
  SB();
  b8 f1049 = cat(l1049, h1049);
  MA(cF110, f1049, asb(x9_0)); SB();
  s4 l1052 = TR(pW1, 40960);
  SB();
  { unsigned t_; asm volatile("s_add_u32 m0, %[wb], 16384\n\ts_mov_b32 %[t], 896\n\tbuffer_load_dwordx4 %[vX], %[rX], %[t] offen lds" : [t]"=&s"(t_) : [wb]"s"(wb), [vX]"v"(vX), [rX]"s"(rX) : "memory", "scc"); }
  SB();
  MA(cF111, f1049, asb(x9_1)); SB();
  s4 h1052 = TR(pW1, 41472);
  SB();
  b8 f1050 = cat(l1050, h1050);
  MA(cH0, f1050, asb(x9_0)); SB();
  s4 l1053 = TR(pW0, 8192);
  SB();
  MA(cH1, f1050, asb(x9_1)); SB();
  s4 h1053 = TR(pW0, 8704);
  SB();
  b8 f1051 = cat(l1051, h1051);
  MA(cF000, f1051, asb(x10_0)); SB();
  s4 l1054 = TR(pW1, 8192);
  SB();
  { unsigned t_; asm volatile("s_add_u32 m0, %[wb], 24576\n\ts_mov_b32 %[t], 74624\n\tbuffer_load_dwordx4 %[vX], %[rX], %[t] offen lds" : [t]"=&s"(t_) : [wb]"s"(wb), [vX]"v"(vX), [rX]"s"(rX) : "memory", "scc"); }
  SB();
  MA(cF001, f1051, asb(x10_1)); SB();
  s4 h1054 = TR(pW1, 8704);
  SB();
  b8 f1052 = cat(l1052, h1052);
  MA(cF010, f1052, asb(x10_0)); SB();
  s4 l1055 = TR(pWh, 0);
  SB();
  MA(cF011, f1052, asb(x10_1)); SB();
  s4 h1055 = TR(pWh, 512);
  SB();
  b8 f1053 = cat(l1053, h1053);
  MA(cF100, f1053, asb(x10_0)); SB();
  const u4 x11_0 = LR(pX10, 49152);
  const u4 x11_1 = LR(pX11, 49152);
  SB();
  { unsigned t_; asm volatile("s_add_u32 m0, %[wb], 114688\n\ts_mov_b32 %[t], 4128768\n\tbuffer_load_dwordx4 %[vW1], %[rW1], %[t] offen lds" : [t]"=&s"(t_) : [wb]"s"(wb), [vW1]"v"(vW1), [rW1]"s"(rW1) : "memory", "scc"); }
  SB();
  MA(cF101, f1053, asb(x10_1)); SB();
  s4 l1056 = TR(pW0, 45056);
  s4 h1056 = TR(pW0, 45568);
  SB();
  b8 f1054 = cat(l1054, h1054);
  MA(cF110, f1054, asb(x10_0)); SB();
  s4 l1057 = TR(pW1, 45056);
  SB();
  MA(cF111, f1054, asb(x10_1)); SB();
  s4 h1057 = TR(pW1, 45568);
  SB();
  b8 f1055 = cat(l1055, h1055);
  MA(cH0, f1055, asb(x10_0)); SB();
  s4 l1058 = TR(pW0, 12288);
  SB();
  { unsigned t_; asm volatile("s_add_u32 m0, %[wb], 131072\n\ts_mov_b32 %[t], 4128896\n\tbuffer_load_dwordx4 %[vW1], %[rW1], %[t] offen lds" : [t]"=&s"(t_) : [wb]"s"(wb), [vW1]"v"(vW1), [rW1]"s"(rW1) : "memory", "scc"); }
  SB();
  MA(cH1, f1055, asb(x10_1)); SB();
  s4 h1058 = TR(pW0, 12800);
  SB();
  b8 f1056 = cat(l1056, h1056);
  MA(cF000, f1056, asb(x11_0)); SB();
  s4 l1059 = TR(pW1, 12288);
  SB();
  MA(cF001, f1056, asb(x11_1)); SB();
  s4 h1059 = TR(pW1, 12800);
  SB();
  b8 f1057 = cat(l1057, h1057);
  MA(cF010, f1057, asb(x11_0)); SB();
  s4 l1060 = TR(pWh, 4096);
  SB();
  { unsigned t_; asm volatile("s_add_u32 m0, %[wb], 90112\n\ts_mov_b32 %[t], 4128768\n\tbuffer_load_dwordx4 %[vW1], %[rW1h], %[t] offen lds" : [t]"=&s"(t_) : [wb]"s"(wb), [vW1]"v"(vW1), [rW1h]"s"(rW1h) : "memory", "scc"); }
  SB();
  MA(cF011, f1057, asb(x11_1)); SB();
  s4 h1060 = TR(pWh, 4608);
  SB();
  { asm volatile("s_waitcnt vmcnt(6)\n\ts_barrier" :  :  : "memory"); }
  SB();
  b8 f1058 = cat(l1058, h1058);
  MA(cF100, f1058, asb(x11_0)); SB();
  const u4 x12_0 = LR(pX00, 32768);
  const u4 x12_1 = LR(pX01, 32768);
  SB();
  MA(cF101, f1058, asb(x11_1)); SB();
  s4 l1061 = TR(pW0, 0);
  s4 h1061 = TR(pW0, 512);
  SB();
  b8 f1059 = cat(l1059, h1059);
  MA(cF110, f1059, asb(x11_0)); SB();
  s4 l1062 = TR(pW1, 0);
  SB();
  { unsigned t_; asm volatile("s_add_u32 m0, %[wb], 0\n\ts_mov_b32 %[t], 1024\n\tbuffer_load_dwordx4 %[vX], %[rX], %[t] offen lds" : [t]"=&s"(t_) : [wb]"s"(wb), [vX]"v"(vX), [rX]"s"(rX) : "memory", "scc"); }
  SB();
  MA(cF111, f1059, asb(x11_1)); SB();
  s4 h1062 = TR(pW1, 512);
  SB();
  b8 f1060 = cat(l1060, h1060);
  MA(cH0, f1060, asb(x11_0)); SB();
  s4 l1063 = TR(pW0, 49152);
  SB();
  MA(cH1, f1060, asb(x11_1)); SB();
  s4 h1063 = TR(pW0, 49664);
  SB();
  b8 f1061 = cat(l1061, h1061);
  MA(cF000, f1061, asb(x12_0)); SB();
  s4 l1064 = TR(pW1, 49152);
  SB();
  { unsigned t_; asm volatile("s_add_u32 m0, %[wb], 8192\n\ts_mov_b32 %[t], 74752\n\tbuffer_load_dwordx4 %[vX], %[rX], %[t] offen lds" : [t]"=&s"(t_) : [wb]"s"(wb), [vX]"v"(vX), [rX]"s"(rX) : "memory", "scc"); }
  SB();
  MA(cF001, f1061, asb(x12_1)); SB();
  s4 h1064 = TR(pW1, 49664);
  SB();
  b8 f1062 = cat(l1062, h1062);
  MA(cF010, f1062, asb(x12_0)); SB();
  s4 l1065 = TR(pWh, 8192);
  SB();
  MA(cF011, f1062, asb(x12_1)); SB();
  s4 h1065 = TR(pWh, 8704);
  SB();
  b8 f1063 = cat(l1063, h1063);
  MA(cF100, f1063, asb(x12_0)); SB();
  const u4 x13_0 = LR(pX10, 32768);
  const u4 x13_1 = LR(pX11, 32768);
  SB();
  { unsigned t_; asm volatile("s_add_u32 m0, %[wb], 155648\n\ts_mov_b32 %[t], 4718592\n\tbuffer_load_dwordx4 %[vW1], %[rW1], %[t] offen lds" : [t]"=&s"(t_) : [wb]"s"(wb), [vW1]"v"(vW1), [rW1]"s"(rW1) : "memory", "scc"); }
  SB();
  MA(cF101, f1063, asb(x12_1)); SB();
  s4 l1066 = TR(pW0, 4096);
  s4 h1066 = TR(pW0, 4608);
  SB();
  b8 f1064 = cat(l1064, h1064);
  MA(cF110, f1064, asb(x12_0)); SB();
  s4 l1067 = TR(pW1, 4096);
  SB();
  MA(cF111, f1064, asb(x12_1)); SB();
  s4 h1067 = TR(pW1, 4608);
  SB();
  b8 f1065 = cat(l1065, h1065);
  MA(cH0, f1065, asb(x12_0)); SB();
  s4 l1068 = TR(pW0, 53248);
  SB();
  { unsigned t_; asm volatile("s_add_u32 m0, %[wb], 139264\n\ts_mov_b32 %[t], 4718720\n\tbuffer_load_dwordx4 %[vW1], %[rW1], %[t] offen lds" : [t]"=&s"(t_) : [wb]"s"(wb), [vW1]"v"(vW1), [rW1]"s"(rW1) : "memory", "scc"); }
  SB();
  MA(cH1, f1065, asb(x12_1)); SB();
  s4 h1068 = TR(pW0, 53760);
  SB();
  b8 f1066 = cat(l1066, h1066);
  MA(cF000, f1066, asb(x13_0)); SB();
  s4 l1069 = TR(pW1, 53248);
  SB();
  MA(cF001, f1066, asb(x13_1)); SB();
  s4 h1069 = TR(pW1, 53760);
  SB();
  b8 f1067 = cat(l1067, h1067);
  MA(cF010, f1067, asb(x13_0)); SB();
  s4 l1070 = TR(pWh, 12288);
  SB();
  { unsigned t_; asm volatile("s_add_u32 m0, %[wb], 122880\n\ts_mov_b32 %[t], 4718592\n\tbuffer_load_dwordx4 %[vW1], %[rW1h], %[t] offen lds" : [t]"=&s"(t_) : [wb]"s"(wb), [vW1]"v"(vW1), [rW1h]"s"(rW1h) : "memory", "scc"); }
  SB();
  MA(cF011, f1067, asb(x13_1)); SB();
  s4 h1070 = TR(pWh, 12800);
  SB();
  { asm volatile("s_waitcnt vmcnt(5)\n\ts_barrier" :  :  : "memory"); }
  SB();
  { asm volatile("s_waitcnt vmcnt(10)" : [pFW]"+v"(pFW) :  : "memory"); }
  repoll<0>(pFW, rWS, v16, sFW, T_); SB();
  SB();
  SB();
  b8 f1068 = cat(l1068, h1068);
  MA(cF100, f1068, asb(x13_0)); SB();
  const u4 x14_0 = LR(pX00, 16384);
  const u4 x14_1 = LR(pX01, 16384);
  SB();
  MA(cF101, f1068, asb(x13_1)); SB();
  s4 l1071 = TR(pW0, 16384);
  s4 h1071 = TR(pW0, 16896);
  SB();
  b8 f1069 = cat(l1069, h1069);
  MA(cF110, f1069, asb(x13_0)); SB();
  s4 l1072 = TR(pW1, 16384);
  SB();
  { unsigned t_; asm volatile("s_add_u32 m0, %[wb], 81920\n\ts_mov_b32 %[t], 0\n\tbuffer_load_dwordx4 %[vL], %[rWQ], %[t] offen sc1 lds" : [t]"=&s"(t_) : [wb]"s"(wb), [vL]"v"(vL), [rWQ]"s"(rWQ) : "memory", "scc"); }
  SB();
  MA(cF111, f1069, asb(x13_1)); SB();
  s4 h1072 = TR(pW1, 16896);
  SB();
  b8 f1070 = cat(l1070, h1070);
  MA(cH0, f1070, asb(x13_0)); SB();
  s4 l1073 = TR(pW0, 32768);
  SB();
  MA(cH1, f1070, asb(x13_1)); SB();
  s4 h1073 = TR(pW0, 33280);
  SB();
  b8 f1071 = cat(l1071, h1071);
  MA(cF000, f1071, asb(x14_0)); SB();
  s4 l1074 = TR(pW1, 32768);
  SB();
  { unsigned t_; asm volatile("s_add_u32 m0, %[wb], 49152\n\ts_mov_b32 %[t], 8192\n\tbuffer_load_dwordx4 %[vL], %[rWQ], %[t] offen sc1 lds" : [t]"=&s"(t_) : [wb]"s"(wb), [vL]"v"(vL), [rWQ]"s"(rWQ) : "memory", "scc"); }
  SB();
  MA(cF001, f1071, asb(x14_1)); SB();
  s4 h1074 = TR(pW1, 33280);
  SB();
  b8 f1072 = cat(l1072, h1072);
  MA(cF010, f1072, asb(x14_0)); SB();
  s4 l1075 = TR(pWh, 24576);
  SB();
  MA(cF011, f1072, asb(x14_1)); SB();
  s4 h1075 = TR(pWh, 25088);
  SB();
  b8 f1073 = cat(l1073, h1073);
  MA(cF100, f1073, asb(x14_0)); SB();
  const u4 x15_0 = LR(pX10, 16384);
  const u4 x15_1 = LR(pX11, 16384);
  SB();
  { unsigned t_; asm volatile("s_add_u32 m0, %[wb], 57344\n\ts_mov_b32 %[t], 16384\n\tbuffer_load_dwordx4 %[vL], %[rWQ], %[t] offen sc1 lds" : [t]"=&s"(t_) : [wb]"s"(wb), [vL]"v"(vL), [rWQ]"s"(rWQ) : "memory", "scc"); }
  SB();
  MA(cF101, f1073, asb(x14_1)); SB();
  s4 l1076 = TR(pW0, 20480);
  s4 h1076 = TR(pW0, 20992);
  SB();
  b8 f1074 = cat(l1074, h1074);
  MA(cF110, f1074, asb(x14_0)); SB();
  s4 l1077 = TR(pW1, 20480);
  SB();
  MA(cF111, f1074, asb(x14_1)); SB();
  s4 h1077 = TR(pW1, 20992);
  SB();
  b8 f1075 = cat(l1075, h1075);
  MA(cH0, f1075, asb(x14_0)); SB();
  s4 l1078 = TR(pW0, 36864);
  SB();
  { unsigned t_; asm volatile("s_add_u32 m0, %[wb], 98304\n\ts_mov_b32 %[t], 24576\n\tbuffer_load_dwordx4 %[vL], %[rWQ], %[t] offen sc1 lds" : [t]"=&s"(t_) : [wb]"s"(wb), [vL]"v"(vL), [rWQ]"s"(rWQ) : "memory", "scc"); }
  SB();
  MA(cH1, f1075, asb(x14_1)); SB();
  s4 h1078 = TR(pW0, 37376);
  SB();
  b8 f1076 = cat(l1076, h1076);
  MA(cF000, f1076, asb(x15_0)); SB();
  s4 l1079 = TR(pW1, 36864);
  SB();
  MA(cF001, f1076, asb(x15_1)); SB();
  s4 h1079 = TR(pW1, 37376);
  SB();
  b8 f1077 = cat(l1077, h1077);
  MA(cF010, f1077, asb(x15_0)); SB();
  s4 l1080 = TR(pWh, 28672);
  SB();
  { unsigned t_; asm volatile("s_add_u32 m0, %[wb], 106496\n\ts_mov_b32 %[t], 32768\n\tbuffer_load_dwordx4 %[vL], %[rWQ], %[t] offen sc1 lds" : [t]"=&s"(t_) : [wb]"s"(wb), [vL]"v"(vL), [rWQ]"s"(rWQ) : "memory", "scc"); }
  SB();
  MA(cF011, f1077, asb(x15_1)); SB();
  s4 h1080 = TR(pWh, 29184);
  SB();
  { asm volatile("s_waitcnt vmcnt(5)\n\ts_barrier" :  :  : "memory"); }
  SB();
  b8 f1078 = cat(l1078, h1078);
  MA(cF100, f1078, asb(x15_0)); SB();
  const u4 x16_0 = LR(pX00, 0);
  const u4 x16_1 = LR(pX01, 0);
  SB();
  MA(cF101, f1078, asb(x15_1)); SB();
  s4 l1081 = TR(pW0, 57344);
  s4 h1081 = TR(pW0, 57856);
  SB();
  b8 f1079 = cat(l1079, h1079);
  MA(cF110, f1079, asb(x15_0)); SB();
  s4 l1082 = TR(pW1, 57344);
  SB();
  { unsigned t_; asm volatile("s_add_u32 m0, %[wb], 65536\n\ts_mov_b32 %[t], 40960\n\tbuffer_load_dwordx4 %[vL], %[rWQ], %[t] offen sc1 lds" : [t]"=&s"(t_) : [wb]"s"(wb), [vL]"v"(vL), [rWQ]"s"(rWQ) : "memory", "scc"); }
  SB();
  MA(cF111, f1079, asb(x15_1)); SB();
  s4 h1082 = TR(pW1, 57856);
  SB();
  b8 f1080 = cat(l1080, h1080);
  MA(cH0, f1080, asb(x15_0)); SB();
  s4 l1083 = TR(pW0, 40960);
  SB();
  MA(cH1, f1080, asb(x15_1)); SB();
  s4 h1083 = TR(pW0, 41472);
  SB();
  b8 f1081 = cat(l1081, h1081);
  MA(cF000, f1081, asb(x16_0)); SB();
  s4 l1084 = TR(pW1, 40960);
  SB();
  { unsigned t_; asm volatile("s_add_u32 m0, %[wb], 40960\n\ts_mov_b32 %[t], 49152\n\tbuffer_load_dwordx4 %[vL], %[rWQ], %[t] offen sc1 lds" : [t]"=&s"(t_) : [wb]"s"(wb), [vL]"v"(vL), [rWQ]"s"(rWQ) : "memory", "scc"); }
  SB();
  MA(cF001, f1081, asb(x16_1)); SB();
  s4 h1084 = TR(pW1, 41472);
  SB();
  b8 f1082 = cat(l1082, h1082);
  MA(cF010, f1082, asb(x16_0)); SB();
  s4 l1085 = TR(pWh, 57344);
  SB();
  MA(cF011, f1082, asb(x16_1)); SB();
  s4 h1085 = TR(pWh, 57856);
  SB();
  b8 f1083 = cat(l1083, h1083);
  MA(cF100, f1083, asb(x16_0)); SB();
  const u4 x17_0 = LR(pX10, 0);
  const u4 x17_1 = LR(pX11, 0);
  SB();
  { unsigned t_; asm volatile("s_add_u32 m0, %[wb], 32768\n\ts_mov_b32 %[t], 57344\n\tbuffer_load_dwordx4 %[vL], %[rWQ], %[t] offen sc1 lds" : [t]"=&s"(t_) : [wb]"s"(wb), [vL]"v"(vL), [rWQ]"s"(rWQ) : "memory", "scc"); }
  SB();
  MA(cF101, f1083, asb(x16_1)); SB();
  s4 l1086 = TR(pW0, 61440);
  s4 h1086 = TR(pW0, 61952);
  SB();
  b8 f1084 = cat(l1084, h1084);
  MA(cF110, f1084, asb(x16_0)); SB();
  s4 l1087 = TR(pW1, 61440);
  SB();
  MA(cF111, f1084, asb(x16_1)); SB();
  s4 h1087 = TR(pW1, 61952);
  SB();
  b8 f1085 = cat(l1085, h1085);
  MA(cH0, f1085, asb(x16_0)); SB();
  s4 l1088 = TR(pW0, 45056);
  SB();
  { unsigned t_; asm volatile("s_add_u32 m0, %[wb], 73728\n\ts_mov_b32 %[t], 65536\n\tbuffer_load_dwordx4 %[vL], %[rWQ], %[t] offen sc1 lds" : [t]"=&s"(t_) : [wb]"s"(wb), [vL]"v"(vL), [rWQ]"s"(rWQ) : "memory", "scc"); }
  SB();
  MA(cH1, f1085, asb(x16_1)); SB();
  s4 h1088 = TR(pW0, 45568);
  SB();
  b8 f1086 = cat(l1086, h1086);
  MF(cF000, f1086, asb(x17_0)); SB();
  s4 l1089 = TR(pW1, 45056);
  SB();
  MF(cF001, f1086, asb(x17_1)); SB();
  s4 h1089 = TR(pW1, 45568);
  SB();
  b8 f1087 = cat(l1087, h1087);
  MF(cF010, f1087, asb(x17_0)); SB();
  s4 l1090 = TR(pWh, 61440);
  SB();
  { unsigned t_; asm volatile("s_add_u32 m0, %[wb], 24576\n\ts_mov_b32 %[t], 73728\n\tbuffer_load_dwordx4 %[vL], %[rWQ], %[t] offen sc1 lds" : [t]"=&s"(t_) : [wb]"s"(wb), [vL]"v"(vL), [rWQ]"s"(rWQ) : "memory", "scc"); }
  SB();
  MF(cF011, f1087, asb(x17_1)); SB();
  s4 h1090 = TR(pWh, 61952);
  SB();
  { asm volatile("s_waitcnt vmcnt(5)\n\ts_barrier" :  :  : "memory"); }
  SB();
  b8 f1088 = cat(l1088, h1088);
  MF(cF100, f1088, asb(x17_0)); SB();
  pW0 += 4294918144u; OPQ(pW0);
  s4 l1091 = TR(pW0, 32768);
  SB();
  { asm volatile("s_waitcnt vmcnt(34)" : [tc2]"+v"(tc2) :  : "memory"); }
  SB();
  { asm volatile("s_waitcnt vmcnt(24)" : [pk0]"+v"(pk0), [pk1]"+v"(pk1) :  : "memory"); }
  SB();
  st16(pk0, rPK, po0, sd0);
  SB();
  st16(pk1, rPK, po1, sd1);
  SB();
  MF(cF101, f1088, asb(x17_1)); SB();
  s4 h1091 = TR(pW0, 33280);
  SB();
  b8 f1089 = cat(l1089, h1089);
  MF(cF110, f1089, asb(x17_0)); SB();
  pW1 += 4294918144u; OPQ(pW1);
  s4 l1092 = TR(pW1, 32768);
  SB();
  { unsigned t_; asm volatile("s_add_u32 m0, %[wb], 16384\n\ts_mov_b32 %[t], 81920\n\tbuffer_load_dwordx4 %[vL], %[rWQ], %[t] offen sc1 lds" : [t]"=&s"(t_) : [wb]"s"(wb), [vL]"v"(vL), [rWQ]"s"(rWQ) : "memory", "scc"); }
  SB();
  MF(cF111, f1089, asb(x17_1)); SB();
  s4 h1092 = TR(pW1, 33280);
  SB();
  b8 f1090 = cat(l1090, h1090);
  MF(cH0, f1090, asb(x17_0)); SB();
  s4 l1093 = TR(pW0, 36864);
  SB();
  MF(cH1, f1090, asb(x17_1)); SB();
  s4 h1093 = TR(pW0, 37376);
  SB();
  SB();
  b8 f1091 = cat(l1091, h1091);
  MZ(cL000, f1091, asb(x0_0)); SB();
  s4 l1094 = TR(pW1, 36864);
  SB();
  { unsigned t_; asm volatile("s_add_u32 m0, %[wb], 114688\n\ts_mov_b32 %[t], 90112\n\tbuffer_load_dwordx4 %[vL], %[rWQ], %[t] offen sc1 lds" : [t]"=&s"(t_) : [wb]"s"(wb), [vL]"v"(vL), [rWQ]"s"(rWQ) : "memory", "scc"); }
  SB();
  MZ(cL001, f1091, asb(x0_1)); SB();
  s4 h1094 = TR(pW1, 37376);
  SB();
  b8 f1092 = cat(l1092, h1092);
  MZ(cL010, f1092, asb(x0_0)); SB();
  s4 l1095 = TR(pW0, 0);
  s4 h1095 = TR(pW0, 512);
  SB();
  float u3001_0 = cF000[0] * -1.4426950408889634f; OPQ(u3001_0); SB(); float v3001_0 = cF000[1] * -1.4426950408889634f; OPQ(v3001_0);
  SB();
  u3001_0 = __builtin_amdgcn_exp2f(u3001_0); OPQ(u3001_0);
  SB();
  b8 f1093 = cat(l1093, h1093);
  MA(cL000, f1093, asb(x1_0)); SB();
  s4 l1096 = TR(pW1, 0);
  SB();
  v3001_0 = __builtin_amdgcn_exp2f(v3001_0); OPQ(v3001_0);
  SB();
  MA(cL001, f1093, asb(x1_1)); SB();
  s4 h1096 = TR(pW1, 512);
  SB();
  u3001_0 += 1.f; OPQ(u3001_0); SB(); v3001_0 += 1.f; OPQ(v3001_0);
  SB();
  b8 f1094 = cat(l1094, h1094);
  MA(cL010, f1094, asb(x1_0)); SB();
  s4 l1097 = TR(pW0, 4096);
  s4 h1097 = TR(pW0, 4608);
  SB();
  u3001_0 = __builtin_amdgcn_rcpf(u3001_0); OPQ(u3001_0);
  SB();
  b8 f1095 = cat(l1095, h1095);
  MA(cL000, f1095, asb(x2_0)); SB();
  s4 l1098 = TR(pW1, 4096);
  SB();
  { unsigned t_; asm volatile("s_add_u32 m0, %[wb], 147456\n\ts_mov_b32 %[t], 98304\n\tbuffer_load_dwordx4 %[vL], %[rWQ], %[t] offen sc1 lds" : [t]"=&s"(t_) : [wb]"s"(wb), [vL]"v"(vL), [rWQ]"s"(rWQ) : "memory", "scc"); }
  SB();
  MA(cL001, f1095, asb(x2_1)); SB();
  s4 h1098 = TR(pW1, 4608);
  SB();
  v3001_0 = __builtin_amdgcn_rcpf(v3001_0); OPQ(v3001_0);
  SB();
  u3001_0 *= cF000[0]; OPQ(u3001_0);
  SB();
  b8 f1096 = cat(l1096, h1096);
  MA(cL010, f1096, asb(x2_0)); SB();
  s4 l1099 = TR(pW0, 8192);
  s4 h1099 = TR(pW0, 8704);
  SB();
  unsigned z3001_0 = pk(u3001_0, v3001_0 * cF000[1]); OPQ(z3001_0);
  SB();
  b8 f1097 = cat(l1097, h1097);
  MA(cL000, f1097, asb(x3_0)); SB();
  s4 l1100 = TR(pW1, 8192);
  SB();
  float u3001_1 = cF000[2] * -1.4426950408889634f; OPQ(u3001_1); SB(); float v3001_1 = cF000[3] * -1.4426950408889634f; OPQ(v3001_1);
  SB();
  MA(cL001, f1097, asb(x3_1)); SB();
  s4 h1100 = TR(pW1, 8704);
  SB();
  u3001_1 = __builtin_amdgcn_exp2f(u3001_1); OPQ(u3001_1);
  SB();
  b8 f1098 = cat(l1098, h1098);
  MA(cL010, f1098, asb(x3_0)); SB();
  s4 l1101 = TR(pW0, 12288);
  s4 h1101 = TR(pW0, 12800);
  SB();
  v3001_1 = __builtin_amdgcn_exp2f(v3001_1); OPQ(v3001_1);
  SB();
  b8 f1099 = cat(l1099, h1099);
  MA(cL000, f1099, asb(x4_0)); SB();
  s4 l1102 = TR(pW1, 12288);
  SB();
  { unsigned t_; asm volatile("s_add_u32 m0, %[wb], 131072\n\ts_mov_b32 %[t], 106496\n\tbuffer_load_dwordx4 %[vL], %[rWQ], %[t] offen sc1 lds" : [t]"=&s"(t_) : [wb]"s"(wb), [vL]"v"(vL), [rWQ]"s"(rWQ) : "memory", "scc"); }
  SB();
  MA(cL001, f1099, asb(x4_1)); SB();
  s4 h1102 = TR(pW1, 12800);
  SB();
  u3001_1 += 1.f; OPQ(u3001_1); SB(); v3001_1 += 1.f; OPQ(v3001_1);
  SB();
  b8 f1100 = cat(l1100, h1100);
  MA(cL010, f1100, asb(x4_0)); SB();
  s4 l1103 = TR(pW0, 49152);
  s4 h1103 = TR(pW0, 49664);
  SB();
  u3001_1 = __builtin_amdgcn_rcpf(u3001_1); OPQ(u3001_1);
  SB();
  v3001_1 = __builtin_amdgcn_rcpf(v3001_1); OPQ(v3001_1);
  SB();
  b8 f1101 = cat(l1101, h1101);
  MA(cL000, f1101, asb(x5_0)); SB();
  s4 l1104 = TR(pW1, 49152);
  SB();
  u3001_1 *= cF000[2]; OPQ(u3001_1);
  SB();
  MA(cL001, f1101, asb(x5_1)); SB();
  s4 h1104 = TR(pW1, 49664);
  SB();
  unsigned z3001_1 = pk(u3001_1, v3001_1 * cF000[3]); OPQ(z3001_1);
  SB();
  b8 f1102 = cat(l1102, h1102);
  MA(cL010, f1102, asb(x5_0)); SB();
  s4 l1105 = TR(pW0, 53248);
  s4 h1105 = TR(pW0, 53760);
  SB();
  float u3001_2 = cF010[0] * -1.4426950408889634f; OPQ(u3001_2); SB(); float v3001_2 = cF010[1] * -1.4426950408889634f; OPQ(v3001_2);
  SB();
  b8 f1103 = cat(l1103, h1103);
  MA(cL000, f1103, asb(x6_0)); SB();
  s4 l1106 = TR(pW1, 53248);
  SB();
  { unsigned t_; asm volatile("s_add_u32 m0, %[wb], 155648\n\ts_mov_b32 %[t], 114688\n\tbuffer_load_dwordx4 %[vL], %[rWQ], %[t] offen sc1 lds" : [t]"=&s"(t_) : [wb]"s"(wb), [vL]"v"(vL), [rWQ]"s"(rWQ) : "memory", "scc"); }
  SB();
  MA(cL001, f1103, asb(x6_1)); SB();
  s4 h1106 = TR(pW1, 53760);
  SB();
  u3001_2 = __builtin_amdgcn_exp2f(u3001_2); OPQ(u3001_2);
  SB();
  v3001_2 = __builtin_amdgcn_exp2f(v3001_2); OPQ(v3001_2);
  SB();
  b8 f1104 = cat(l1104, h1104);
  MA(cL010, f1104, asb(x6_0)); SB();
  s4 l1107 = TR(pW0, 57344);
  s4 h1107 = TR(pW0, 57856);
  SB();
  u3001_2 += 1.f; OPQ(u3001_2); SB(); v3001_2 += 1.f; OPQ(v3001_2);
  SB();
  b8 f1105 = cat(l1105, h1105);
  MA(cL000, f1105, asb(x7_0)); SB();
  s4 l1108 = TR(pW1, 57344);
  SB();
  u3001_2 = __builtin_amdgcn_rcpf(u3001_2); OPQ(u3001_2);
  SB();
  MA(cL001, f1105, asb(x7_1)); SB();
  s4 h1108 = TR(pW1, 57856);
  SB();
  v3001_2 = __builtin_amdgcn_rcpf(v3001_2); OPQ(v3001_2);
  SB();
  b8 f1106 = cat(l1106, h1106);
  MA(cL010, f1106, asb(x7_0)); SB();
  s4 l1109 = TR(pW0, 61440);
  s4 h1109 = TR(pW0, 61952);
  SB();
  b8 f1107 = cat(l1107, h1107);
  MA(cL000, f1107, asb(x8_0)); SB();
  s4 l1110 = TR(pW1, 61440);
  SB();
  unsigned tc3 = __umul24(v16 & 16, 82944) + vW2; asm volatile("v_writelane_b32 %0, %1, 62\n\tv_writelane_b32 %0, %2, 63" : "+v"(tc3) : "s"(sx3), "s"(sx3 + 1327104));
  { asm volatile("buffer_load_dword %[tc3], %[tc3], %[rW2], %[st3] offen" : [tc3]"+v"(tc3) : [rW2]"s"(rW2), [st3]"s"(st3) : "memory"); }
  SB();
  u3001_2 *= cF010[0]; OPQ(u3001_2);
  SB();
  MA(cL001, f1107, asb(x8_1)); SB();
  s4 h1110 = TR(pW1, 61952);
  SB();
  unsigned z3001_2 = pk(u3001_2, v3001_2 * cF010[1]); OPQ(z3001_2);
  SB();
  { asm volatile("s_waitcnt vmcnt(8)\n\ts_barrier" :  :  : "memory"); }
  SB();
  b8 f1108 = cat(l1108, h1108);
  MA(cL010, f1108, asb(x8_0)); SB();
  s4 l1111 = TR(pW0, 16384);
  s4 h1111 = TR(pW0, 16896);
  SB();
  float u3001_3 = cF010[2] * -1.4426950408889634f; OPQ(u3001_3); SB(); float v3001_3 = cF010[3] * -1.4426950408889634f; OPQ(v3001_3);
  SB();
  u3001_3 = __builtin_amdgcn_exp2f(u3001_3); OPQ(u3001_3);
  SB();
  b8 f1109 = cat(l1109, h1109);
  MA(cL000, f1109, asb(x9_0)); SB();
  s4 l1112 = TR(pW1, 16384);
  SB();
  v3001_3 = __builtin_amdgcn_exp2f(v3001_3); OPQ(v3001_3);
  SB();
  MA(cL001, f1109, asb(x9_1)); SB();
  s4 h1112 = TR(pW1, 16896);
  SB();
  u3001_3 += 1.f; OPQ(u3001_3); SB(); v3001_3 += 1.f; OPQ(v3001_3);
  SB();
  b8 f1110 = cat(l1110, h1110);
  MA(cL010, f1110, asb(x9_0)); SB();
  s4 l1113 = TR(pW0, 20480);
  s4 h1113 = TR(pW0, 20992);
  SB();
  u3001_3 = __builtin_amdgcn_rcpf(u3001_3); OPQ(u3001_3);
  SB();
  b8 f1111 = cat(l1111, h1111);
  MA(cL000, f1111, asb(x10_0)); SB();
  s4 l1114 = TR(pW1, 20480);
  SB();
  { unsigned t_; asm volatile("s_add_u32 m0, %[wb], 0\n\ts_mov_b32 %[t], 122880\n\tbuffer_load_dwordx4 %[vL], %[rWQ], %[t] offen sc1 lds" : [t]"=&s"(t_) : [wb]"s"(wb), [vL]"v"(vL), [rWQ]"s"(rWQ) : "memory", "scc"); }
  SB();
  MA(cL001, f1111, asb(x10_1)); SB();
  s4 h1114 = TR(pW1, 20992);
  SB();
  v3001_3 = __builtin_amdgcn_rcpf(v3001_3); OPQ(v3001_3);
  SB();
  u3001_3 *= cF010[2]; OPQ(u3001_3);
  SB();
  b8 f1112 = cat(l1112, h1112);
  MA(cL010, f1112, asb(x10_0)); SB();
  pW0 += 4294934528u; OPQ(pW0);
  s4 l1115 = TR(pW0, 24576);
  s4 h1115 = TR(pW0, 25088);
  SB();
  unsigned z3001_3 = pk(u3001_3, v3001_3 * cF010[3]); OPQ(z3001_3);
  SB();
  b8 f1113 = cat(l1113, h1113);
  MA(cL000, f1113, asb(x11_0)); SB();
  pW1 += 4294934528u; OPQ(pW1);
  s4 l1116 = TR(pW1, 24576);
  SB();
  u4 hk00 = (u4){z3001_0, z3001_1, z3001_2, z3001_3}; OPQ(hk00);
  st16(hk00, rWS, v16, sH + (mF + 0) * 16384 + cg * 8192 + 0);
  SB();
  MA(cL001, f1113, asb(x11_1)); SB();
  s4 h1116 = TR(pW1, 25088);
  SB();
  float u3002_0 = cF001[0] * -1.4426950408889634f; OPQ(u3002_0); SB(); float v3002_0 = cF001[1] * -1.4426950408889634f; OPQ(v3002_0);
  SB();
  b8 f1114 = cat(l1114, h1114);
  MA(cL010, f1114, asb(x11_0)); SB();
  s4 l1117 = TR(pW0, 28672);
  s4 h1117 = TR(pW0, 29184);
  SB();
  u3002_0 = __builtin_amdgcn_exp2f(u3002_0); OPQ(u3002_0);
  SB();
  v3002_0 = __builtin_amdgcn_exp2f(v3002_0); OPQ(v3002_0);
  SB();
  b8 f1115 = cat(l1115, h1115);
  MA(cL000, f1115, asb(x12_0)); SB();
  s4 l1118 = TR(pW1, 28672);
  SB();
  { unsigned t_; asm volatile("s_add_u32 m0, %[wb], 8192\n\ts_mov_b32 %[t], 131072\n\tbuffer_load_dwordx4 %[vL], %[rWQ], %[t] offen sc1 lds" : [t]"=&s"(t_) : [wb]"s"(wb), [vL]"v"(vL), [rWQ]"s"(rWQ) : "memory", "scc"); }
  SB();
  MA(cL001, f1115, asb(x12_1)); SB();
  s4 h1118 = TR(pW1, 29184);
  SB();
  u3002_0 += 1.f; OPQ(u3002_0); SB(); v3002_0 += 1.f; OPQ(v3002_0);
  SB();
  b8 f1116 = cat(l1116, h1116);
  MA(cL010, f1116, asb(x12_0)); SB();
  s4 l1119 = TR(pW0, 16384);
  s4 h1119 = TR(pW0, 16896);
  SB();
  u3002_0 = __builtin_amdgcn_rcpf(u3002_0); OPQ(u3002_0);
  SB();
  b8 f1117 = cat(l1117, h1117);
  MA(cL000, f1117, asb(x13_0)); SB();
  s4 l1120 = TR(pW1, 16384);
  SB();
  v3002_0 = __builtin_amdgcn_rcpf(v3002_0); OPQ(v3002_0);
  SB();
  MA(cL001, f1117, asb(x13_1)); SB();
  s4 h1120 = TR(pW1, 16896);
  SB();
  u3002_0 *= cF001[0]; OPQ(u3002_0);
  SB();
  unsigned z3002_0 = pk(u3002_0, v3002_0 * cF001[1]); OPQ(z3002_0);
  SB();
  b8 f1118 = cat(l1118, h1118);
  MA(cL010, f1118, asb(x13_0)); SB();
  s4 l1121 = TR(pW0, 20480);
  s4 h1121 = TR(pW0, 20992);
  SB();
  float u3002_1 = cF001[2] * -1.4426950408889634f; OPQ(u3002_1); SB(); float v3002_1 = cF001[3] * -1.4426950408889634f; OPQ(v3002_1);
  SB();
  b8 f1119 = cat(l1119, h1119);
  MA(cL000, f1119, asb(x14_0)); SB();
  s4 l1122 = TR(pW1, 20480);
  SB();
  { unsigned t_; asm volatile("s_add_u32 m0, %[wb], 57344\n\ts_mov_b32 %[t], 139264\n\tbuffer_load_dwordx4 %[vL], %[rWQ], %[t] offen sc1 lds" : [t]"=&s"(t_) : [wb]"s"(wb), [vL]"v"(vL), [rWQ]"s"(rWQ) : "memory", "scc"); }
  SB();
  MA(cL001, f1119, asb(x14_1)); SB();
  s4 h1122 = TR(pW1, 20992);
  SB();
  u3002_1 = __builtin_amdgcn_exp2f(u3002_1); OPQ(u3002_1);
  SB();
  b8 f1120 = cat(l1120, h1120);
  MA(cL010, f1120, asb(x14_0)); SB();
  s4 l1123 = TR(pW0, 57344);
  s4 h1123 = TR(pW0, 57856);
  SB();
  v3002_1 = __builtin_amdgcn_exp2f(v3002_1); OPQ(v3002_1);
  SB();
  b8 f1121 = cat(l1121, h1121);
  MA(cL000, f1121, asb(x15_0)); SB();
  s4 l1124 = TR(pW1, 57344);
  SB();
  u3002_1 += 1.f; OPQ(u3002_1); SB(); v3002_1 += 1.f; OPQ(v3002_1);
  SB();
  MA(cL001, f1121, asb(x15_1)); SB();
  s4 h1124 = TR(pW1, 57856);
  SB();
  u3002_1 = __builtin_amdgcn_rcpf(u3002_1); OPQ(u3002_1);
  SB();
  b8 f1122 = cat(l1122, h1122);
  MA(cL010, f1122, asb(x15_0)); SB();
  s4 l1125 = TR(pW0, 61440);
  s4 h1125 = TR(pW0, 61952);
  SB();
  v3002_1 = __builtin_amdgcn_rcpf(v3002_1); OPQ(v3002_1);
  SB();
  u3002_1 *= cF001[2]; OPQ(u3002_1);
  SB();
  b8 f1123 = cat(l1123, h1123);
  MA(cL000, f1123, asb(x16_0)); SB();
  s4 l1126 = TR(pW1, 61440);
  SB();
  { unsigned t_; asm volatile("s_add_u32 m0, %[wb], 49152\n\ts_mov_b32 %[t], 256\n\tbuffer_load_dwordx4 %[vW1], %[rW1], %[t] offen lds" : [t]"=&s"(t_) : [wb]"s"(wb), [vW1]"v"(vW1), [rW1]"s"(rW1) : "memory", "scc"); }
  SB();
  MA(cL001, f1123, asb(x16_1)); SB();
  s4 h1126 = TR(pW1, 61952);
  SB();
  unsigned z3002_1 = pk(u3002_1, v3002_1 * cF001[3]); OPQ(z3002_1);
  SB();
  b8 f1124 = cat(l1124, h1124);
  MA(cL010, f1124, asb(x16_0)); SB();
  s4 l1127 = TR(pW0, 8192);
  s4 h1127 = TR(pW0, 8704);
  SB();
  float u3002_2 = cF011[0] * -1.4426950408889634f; OPQ(u3002_2); SB(); float v3002_2 = cF011[1] * -1.4426950408889634f; OPQ(v3002_2);
  SB();
  b8 f1125 = cat(l1125, h1125);
  MF(cL000, f1125, asb(x17_0)); SB();
  s4 l1128 = TR(pW1, 8192);
  SB();
  u3002_2 = __builtin_amdgcn_exp2f(u3002_2); OPQ(u3002_2);
  SB();
  MF(cL001, f1125, asb(x17_1)); SB();
  s4 h1128 = TR(pW1, 8704);
  SB();
  v3002_2 = __builtin_amdgcn_exp2f(v3002_2); OPQ(v3002_2);
  SB();
  b8 f1126 = cat(l1126, h1126);
  MF(cL010, f1126, asb(x17_0)); SB();
  s4 l1129 = TR(pW0, 12288);
  s4 h1129 = TR(pW0, 12800);
  SB();
  u3002_2 += 1.f; OPQ(u3002_2); SB(); v3002_2 += 1.f; OPQ(v3002_2);
  SB();
  SB();
  b8 f1127 = cat(l1127, h1127);
  MZ(cL100, f1127, asb(x0_0)); SB();
  s4 l1130 = TR(pW1, 12288);
  SB();
  { unsigned t_; asm volatile("s_add_u32 m0, %[wb], 139264\n\ts_mov_b32 %[t], 590080\n\tbuffer_load_dwordx4 %[vW1], %[rW1], %[t] offen lds" : [t]"=&s"(t_) : [wb]"s"(wb), [vW1]"v"(vW1), [rW1]"s"(rW1) : "memory", "scc"); }
  SB();
  MZ(cL101, f1127, asb(x0_1)); SB();
  s4 h1130 = TR(pW1, 12800);
  SB();
  u3002_2 = __builtin_amdgcn_rcpf(u3002_2); OPQ(u3002_2);
  SB();
  v3002_2 = __builtin_amdgcn_rcpf(v3002_2); OPQ(v3002_2);
  SB();
  { asm volatile("s_waitcnt vmcnt(7)\n\ts_barrier" :  :  : "memory"); }
  SB();
  st8((u2){tlo, thi}, rWS, vF + 0, sGX); SB();
  SB();
  b8 f1128 = cat(l1128, h1128);
  MZ(cL110, f1128, asb(x0_0)); SB();
  s4 l1131 = TR(pW0, 0);
  SB();
  u3002_2 *= cF011[0]; OPQ(u3002_2);
  SB();
  MZ(cL111, f1128, asb(x0_1)); SB();
  s4 h1131 = TR(pW0, 512);
  SB();
  unsigned z3002_2 = pk(u3002_2, v3002_2 * cF011[1]); OPQ(z3002_2);
  SB();
  b8 f1129 = cat(l1129, h1129);
  MA(cL100, f1129, asb(x1_0)); SB();
  s4 l1132 = TR(pW1, 0);
  SB();
  float gs = -1.4426950408889634f * rb(cL010[0]); OPQ(gs);
  SB();
  MA(cL101, f1129, asb(x1_1)); SB();
  s4 h1132 = TR(pW1, 512);
  SB();
  gs = __builtin_amdgcn_exp2f(gs); OPQ(gs);
  SB();
  b8 f1130 = cat(l1130, h1130);
  MA(cL110, f1130, asb(x1_0)); SB();
  s4 l1133 = TR(pW0, 4096);
  SB();
  { unsigned t_; asm volatile("s_add_u32 m0, %[wb], 98304\n\ts_mov_b32 %[t], 1179904\n\tbuffer_load_dwordx4 %[vW1], %[rW1], %[t] offen lds" : [t]"=&s"(t_) : [wb]"s"(wb), [vW1]"v"(vW1), [rW1]"s"(rW1) : "memory", "scc"); }
  SB();
  MA(cL111, f1130, asb(x1_1)); SB();
  s4 h1133 = TR(pW0, 4608);
  SB();
  unsigned xq = pk(cL010[0] * 0.34004648f, cL010[1] * 0.34004648f); OPQ(xq);
  SB();
  b8 f1131 = cat(l1131, h1131);
  MA(cL100, f1131, asb(x2_0)); SB();
  s4 l1134 = TR(pW1, 4096);
  SB();
  gs = __builtin_amdgcn_rcpf(1.f + gs); OPQ(gs);
  SB();
  MA(cL101, f1131, asb(x2_1)); SB();
  s4 h1134 = TR(pW1, 4608);
  SB();
  b8 f1132 = cat(l1132, h1132);
  MA(cL110, f1132, asb(x2_0)); SB();
  pW0 += 81920u; OPQ(pW0);
  s4 l1135 = TR(pW0, 16384);
  SB();
  u2 vp0 = pk4(cL000); OPQ(vp0);
  SB();
  gs = rb(gs); OPQ(gs);
  SB();
  MA(cL111, f1132, asb(x2_1)); SB();
  s4 h1135 = TR(pW0, 16896);
  SB();
  u2 kx = {pk(cL010[2], cL010[3]), LN(0xffff0000ffff0000ull) ? f2u(gs) : xq}; OPQ(kx);
  SB();
  b8 f1133 = cat(l1133, h1133);
  MA(cL100, f1133, asb(x3_0)); SB();
  pW1 += 81920u; OPQ(pW1);
  s4 l1136 = TR(pW1, 16384);
  SB();
  { unsigned t_; asm volatile("s_add_u32 m0, %[wb], 122880\n\ts_mov_b32 %[t], 1769728\n\tbuffer_load_dwordx4 %[vW1], %[rW1], %[t] offen lds" : [t]"=&s"(t_) : [wb]"s"(wb), [vW1]"v"(vW1), [rW1]"s"(rW1) : "memory", "scc"); }
  SB();
  MA(cL101, f1133, asb(x3_1)); SB();
  s4 h1136 = TR(pW1, 16896);
  SB();
  u2 vp1 = pk4(cL001); OPQ(vp1);
  SB();
  b8 f1134 = cat(l1134, h1134);
  MA(cL110, f1134, asb(x3_0)); SB();
  s4 l1137 = TR(pW0, 20480);
  SB();
  *(LDSP u2*)xa = kx;
  SB();
  const unsigned sKV = 7340032u + ((Rb * 16 + c) * 8 + w) * 3072; const bool vs_ = LN(cg ? 0x0f0f0f0f0f0f0f0full : 0xf0f0f0f0f0f0f0f0ull);
  SB();
  MA(cL111, f1134, asb(x3_1)); SB();
  s4 h1137 = TR(pW0, 20992);
  SB();
  u4 vv = vs_ ? cat2(vp1, vp0) : cat2(vp0, vp1); OPQ(vv);
  SB();
  b8 f1135 = cat(l1135, h1135);
  MA(cL100, f1135, asb(x4_0)); SB();
  s4 l1138 = TR(pW1, 20480);
  SB();
  st16(vv, rWS, vo_, sKV + 2048);
  SB();
  LR(lb + 49152 + ((8 + r) * 2 + cg) * 1024 + vo_, 0) = vv;
  SB();
  MA(cL101, f1135, asb(x4_1)); SB();
  s4 h1138 = TR(pW1, 20992);
  SB();
  float u3002_3 = cF011[2] * -1.4426950408889634f; OPQ(u3002_3); SB(); float v3002_3 = cF011[3] * -1.4426950408889634f; OPQ(v3002_3);
  SB();
  b8 f1136 = cat(l1136, h1136);
  MA(cL110, f1136, asb(x4_0)); SB();
  s4 l1139 = TR(pW0, 49152);
  SB();
  { unsigned t_; asm volatile("s_add_u32 m0, %[wb], 106496\n\ts_mov_b32 %[t], 2359552\n\tbuffer_load_dwordx4 %[vW1], %[rW1], %[t] offen lds" : [t]"=&s"(t_) : [wb]"s"(wb), [vW1]"v"(vW1), [rW1]"s"(rW1) : "memory", "scc"); }
  SB();
  MA(cL111, f1136, asb(x4_1)); SB();
  s4 h1139 = TR(pW0, 49664);
  SB();
  u3002_3 = __builtin_amdgcn_exp2f(u3002_3); OPQ(u3002_3);
  SB();
  b8 f1137 = cat(l1137, h1137);
  MA(cL100, f1137, asb(x5_0)); SB();
  s4 l1140 = TR(pW1, 49152);
  SB();
  v3002_3 = __builtin_amdgcn_exp2f(v3002_3); OPQ(v3002_3);
  SB();
  MA(cL101, f1137, asb(x5_1)); SB();
  s4 h1140 = TR(pW1, 49664);
  SB();
  u3002_3 += 1.f; OPQ(u3002_3); SB(); v3002_3 += 1.f; OPQ(v3002_3);
  SB();
  b8 f1138 = cat(l1138, h1138);
  MA(cL110, f1138, asb(x5_0)); SB();
  s4 l1141 = TR(pW0, 53248);
  SB();
  u3002_3 = __builtin_amdgcn_rcpf(u3002_3); OPQ(u3002_3);
  SB();
  MA(cL111, f1138, asb(x5_1)); SB();
  s4 h1141 = TR(pW0, 53760);
  SB();
  v3002_3 = __builtin_amdgcn_rcpf(v3002_3); OPQ(v3002_3);
  SB();
  u3002_3 *= cF011[2]; OPQ(u3002_3);
  SB();
  b8 f1139 = cat(l1139, h1139);
  MA(cL100, f1139, asb(x6_0)); SB();
  s4 l1142 = TR(pW1, 53248);
  SB();
  { unsigned t_; asm volatile("s_add_u32 m0, %[wb], 90112\n\ts_mov_b32 %[t], 2949376\n\tbuffer_load_dwordx4 %[vW1], %[rW1], %[t] offen lds" : [t]"=&s"(t_) : [wb]"s"(wb), [vW1]"v"(vW1), [rW1]"s"(rW1) : "memory", "scc"); }
  SB();
  MA(cL101, f1139, asb(x6_1)); SB();
  s4 h1142 = TR(pW1, 53760);
  SB();
  unsigned z3002_3 = pk(u3002_3, v3002_3 * cF011[3]); OPQ(z3002_3);
  SB();
  u4 hk01 = (u4){z3002_0, z3002_1, z3002_2, z3002_3}; OPQ(hk01);
  st16(hk01, rWS, v16, sH + (mF + 0) * 16384 + cg * 8192 + 1024);
  SB();
  b8 f1140 = cat(l1140, h1140);
  MA(cL110, f1140, asb(x6_0)); SB();
  s4 l1143 = TR(pW0, 32768);
  SB();
  float u3003_0 = cF100[0] * -1.4426950408889634f; OPQ(u3003_0); SB(); float v3003_0 = cF100[1] * -1.4426950408889634f; OPQ(v3003_0);
  SB();
  MA(cL111, f1140, asb(x6_1)); SB();
  s4 h1143 = TR(pW0, 33280);
  SB();
  u3003_0 = __builtin_amdgcn_exp2f(u3003_0); OPQ(u3003_0);
  SB();
  b8 f1141 = cat(l1141, h1141);
  MA(cL100, f1141, asb(x7_0)); SB();
  s4 l1144 = TR(pW1, 32768);
  SB();
  v3003_0 = __builtin_amdgcn_exp2f(v3003_0); OPQ(v3003_0);
  SB();
  MA(cL101, f1141, asb(x7_1)); SB();
  s4 h1144 = TR(pW1, 33280);
  SB();
  u3003_0 += 1.f; OPQ(u3003_0); SB(); v3003_0 += 1.f; OPQ(v3003_0);
  SB();
  b8 f1142 = cat(l1142, h1142);
  MA(cL110, f1142, asb(x7_0)); SB();
  s4 l1145 = TR(pW0, 36864);
  SB();
  { unsigned t_; asm volatile("s_add_u32 m0, %[wb], 81920\n\ts_mov_b32 %[t], 3539200\n\tbuffer_load_dwordx4 %[vW1], %[rW1], %[t] offen lds" : [t]"=&s"(t_) : [wb]"s"(wb), [vW1]"v"(vW1), [rW1]"s"(rW1) : "memory", "scc"); }
  SB();
  MA(cL111, f1142, asb(x7_1)); SB();
  s4 h1145 = TR(pW0, 37376);
  SB();
  u3003_0 = __builtin_amdgcn_rcpf(u3003_0); OPQ(u3003_0);
  SB();
  b8 f1143 = cat(l1143, h1143);
  MA(cL100, f1143, asb(x8_0)); SB();
  s4 l1146 = TR(pW1, 36864);
  SB();
  v3003_0 = __builtin_amdgcn_rcpf(v3003_0); OPQ(v3003_0);
  SB();
  u3003_0 *= cF100[0]; OPQ(u3003_0);
  SB();
  MA(cL101, f1143, asb(x8_1)); SB();
  s4 h1146 = TR(pW1, 37376);
  SB();
  unsigned z3003_0 = pk(u3003_0, v3003_0 * cF100[1]); OPQ(z3003_0);
  SB();
  b8 f1144 = cat(l1144, h1144);
  MA(cL110, f1144, asb(x8_0)); SB();
  s4 l1147 = TR(pW0, 57344);
  SB();
  float u3003_1 = cF100[2] * -1.4426950408889634f; OPQ(u3003_1); SB(); float v3003_1 = cF100[3] * -1.4426950408889634f; OPQ(v3003_1);
  SB();
  MA(cL111, f1144, asb(x8_1)); SB();
  s4 h1147 = TR(pW0, 57856);
  SB();
  u3003_1 = __builtin_amdgcn_exp2f(u3003_1); OPQ(u3003_1);
  SB();
  b8 f1145 = cat(l1145, h1145);
  MA(cL100, f1145, asb(x9_0)); SB();
  s4 l1148 = TR(pW1, 57344);
  SB();
  { unsigned t_; asm volatile("s_add_u32 m0, %[wb], 73728\n\ts_mov_b32 %[t], 4129024\n\tbuffer_load_dwordx4 %[vW1], %[rW1], %[t] offen lds" : [t]"=&s"(t_) : [wb]"s"(wb), [vW1]"v"(vW1), [rW1]"s"(rW1) : "memory", "scc"); }
  SB();
  MA(cL101, f1145, asb(x9_1)); SB();
  s4 h1148 = TR(pW1, 57856);
  SB();
  v3003_1 = __builtin_amdgcn_exp2f(v3003_1); OPQ(v3003_1);
  SB();
  b8 f1146 = cat(l1146, h1146);
  MA(cL110, f1146, asb(x9_0)); SB();
  s4 l1149 = TR(pW0, 61440);
  SB();
  { asm volatile("s_waitcnt vmcnt(15)" : [tc3]"+v"(tc3) :  : "memory"); }
  SB();
  u3003_1 += 1.f; OPQ(u3003_1); SB(); v3003_1 += 1.f; OPQ(v3003_1);
  SB();
  MA(cL111, f1146, asb(x9_1)); SB();
  s4 h1149 = TR(pW0, 61952);
  SB();
  u3003_1 = __builtin_amdgcn_rcpf(u3003_1); OPQ(u3003_1);
  SB();
  v3003_1 = __builtin_amdgcn_rcpf(v3003_1); OPQ(v3003_1);
  SB();
  b8 f1147 = cat(l1147, h1147);
  MA(cL100, f1147, asb(x10_0)); SB();
  s4 l1150 = TR(pW1, 61440);
  SB();
  u3003_1 *= cF100[2]; OPQ(u3003_1);
  SB();
  MA(cL101, f1147, asb(x10_1)); SB();
  s4 h1150 = TR(pW1, 61952);
  SB();
  unsigned z3003_1 = pk(u3003_1, v3003_1 * cF100[3]); OPQ(z3003_1);
  SB();
  { asm volatile("s_waitcnt vmcnt(9)\n\ts_barrier" :  :  : "memory"); }
  SB();
  b8 f1148 = cat(l1148, h1148);
  MA(cL110, f1148, asb(x10_0)); SB();
  pW0 += 4294868992u; OPQ(pW0);
  s4 l1151 = TR(pW0, 0);
  SB();
  { unsigned t_; asm volatile("s_add_u32 m0, %[wb], 24576\n\ts_mov_b32 %[t], 4718848\n\tbuffer_load_dwordx4 %[vW1], %[rW1], %[t] offen lds" : [t]"=&s"(t_) : [wb]"s"(wb), [vW1]"v"(vW1), [rW1]"s"(rW1) : "memory", "scc"); }
  SB();
  MA(cL111, f1148, asb(x10_1)); SB();
  s4 h1151 = TR(pW0, 512);
  SB();
  { asm volatile("s_waitcnt vmcnt(6)" :  :  : "memory"); }
  st8((u2){tlo, thi}, rWS, vF + 256, sGK); SB();
  SB();
  b8 f1149 = cat(l1149, h1149);
  MA(cL100, f1149, asb(x11_0)); SB();
  pW1 += 4294868992u; OPQ(pW1);
  s4 l1152 = TR(pW1, 0);
  SB();
  float u3003_2 = cF110[0] * -1.4426950408889634f; OPQ(u3003_2); SB(); float v3003_2 = cF110[1] * -1.4426950408889634f; OPQ(v3003_2);
  SB();
  MA(cL101, f1149, asb(x11_1)); SB();
  s4 h1152 = TR(pW1, 512);
  SB();
  u3003_2 = __builtin_amdgcn_exp2f(u3003_2); OPQ(u3003_2);
  SB();
  v3003_2 = __builtin_amdgcn_exp2f(v3003_2); OPQ(v3003_2);
  SB();
  b8 f1150 = cat(l1150, h1150);
  MA(cL110, f1150, asb(x11_0)); SB();
  s4 l1153 = TR(pW0, 4096);
  SB();
  u3003_2 += 1.f; OPQ(u3003_2); SB(); v3003_2 += 1.f; OPQ(v3003_2);
  SB();
  MA(cL111, f1150, asb(x11_1)); SB();
  s4 h1153 = TR(pW0, 4608);
  SB();
  u3003_2 = __builtin_amdgcn_rcpf(u3003_2); OPQ(u3003_2);
  SB();
  b8 f1151 = cat(l1151, h1151);
  MA(cL100, f1151, asb(x12_0)); SB();
  s4 l1154 = TR(pW1, 4096);
  SB();
  { unsigned t_; asm volatile("s_add_u32 m0, %[wb], 16384\n\ts_mov_b32 %[t], 384\n\tbuffer_load_dwordx4 %[vW1], %[rW1], %[t] offen lds" : [t]"=&s"(t_) : [wb]"s"(wb), [vW1]"v"(vW1), [rW1]"s"(rW1) : "memory", "scc"); }
  SB();
  MA(cL101, f1151, asb(x12_1)); SB();
  s4 h1154 = TR(pW1, 4608);
  SB();
  v3003_2 = __builtin_amdgcn_rcpf(v3003_2); OPQ(v3003_2);
  SB();
  b8 f1152 = cat(l1152, h1152);
  MA(cL110, f1152, asb(x12_0)); SB();
  s4 l1155 = TR(pW0, 8192);
  SB();
  u3003_2 *= cF110[0]; OPQ(u3003_2);
  SB();
  unsigned z3003_2 = pk(u3003_2, v3003_2 * cF110[1]); OPQ(z3003_2);
  SB();
  MA(cL111, f1152, asb(x12_1)); SB();
  s4 h1155 = TR(pW0, 8704);
  SB();
  float u3003_3 = cF110[2] * -1.4426950408889634f; OPQ(u3003_3); SB(); float v3003_3 = cF110[3] * -1.4426950408889634f; OPQ(v3003_3);
  SB();
  b8 f1153 = cat(l1153, h1153);
  MA(cL100, f1153, asb(x13_0)); SB();
  s4 l1156 = TR(pW1, 8192);
  SB();
  u3003_3 = __builtin_amdgcn_exp2f(u3003_3); OPQ(u3003_3);
  SB();
  MA(cL101, f1153, asb(x13_1)); SB();
  s4 h1156 = TR(pW1, 8704);
  SB();
  v3003_3 = __builtin_amdgcn_exp2f(v3003_3); OPQ(v3003_3);
  SB();
  b8 f1154 = cat(l1154, h1154);
  MA(cL110, f1154, asb(x13_0)); SB();
  s4 l1157 = TR(pW0, 12288);
  SB();
  { unsigned t_; asm volatile("s_add_u32 m0, %[wb], 114688\n\ts_mov_b32 %[t], 590208\n\tbuffer_load_dwordx4 %[vW1], %[rW1], %[t] offen lds" : [t]"=&s"(t_) : [wb]"s"(wb), [vW1]"v"(vW1), [rW1]"s"(rW1) : "memory", "scc"); }
  SB();
  MA(cL111, f1154, asb(x13_1)); SB();
  s4 h1157 = TR(pW0, 12800);
  SB();
  u3003_3 += 1.f; OPQ(u3003_3); SB(); v3003_3 += 1.f; OPQ(v3003_3);
  SB();
  b8 f1155 = cat(l1155, h1155);
  MA(cL100, f1155, asb(x14_0)); SB();
  s4 l1158 = TR(pW1, 12288);
  SB();
  u3003_3 = __builtin_amdgcn_rcpf(u3003_3); OPQ(u3003_3);
  SB();
  MA(cL101, f1155, asb(x14_1)); SB();
  s4 h1158 = TR(pW1, 12800);
  SB();
  v3003_3 = __builtin_amdgcn_rcpf(v3003_3); OPQ(v3003_3);
  SB();
  u3003_3 *= cF110[2]; OPQ(u3003_3);
  SB();
  b8 f1156 = cat(l1156, h1156);
  MA(cL110, f1156, asb(x14_0)); SB();
  s4 l1159 = TR(pW0, 57344);
  SB();
  unsigned z3003_3 = pk(u3003_3, v3003_3 * cF110[3]); OPQ(z3003_3);
  SB();
  MA(cL111, f1156, asb(x14_1)); SB();
  s4 h1159 = TR(pW0, 57856);
  SB();
  u4 hk10 = (u4){z3003_0, z3003_1, z3003_2, z3003_3}; OPQ(hk10);
  st16(hk10, rWS, v16, sH + (mF + 1) * 16384 + cg * 8192 + 0);
  SB();
  float u3004_0 = cF101[0] * -1.4426950408889634f; OPQ(u3004_0); SB(); float v3004_0 = cF101[1] * -1.4426950408889634f; OPQ(v3004_0);
  SB();
  b8 f1157 = cat(l1157, h1157);
  MA(cL100, f1157, asb(x15_0)); SB();
  s4 l1160 = TR(pW1, 57344);
  SB();
  { unsigned t_; asm volatile("s_add_u32 m0, %[wb], 147456\n\ts_mov_b32 %[t], 1180032\n\tbuffer_load_dwordx4 %[vW1], %[rW1], %[t] offen lds" : [t]"=&s"(t_) : [wb]"s"(wb), [vW1]"v"(vW1), [rW1]"s"(rW1) : "memory", "scc"); }
  SB();
  MA(cL101, f1157, asb(x15_1)); SB();
  s4 h1160 = TR(pW1, 57856);
  SB();
  SB();
  unsigned kp1 = v16 + (lb + ((8 + r) * 2 + cg) * 2048 + (cg ^ 1) * 1024), xr = kp1 & ~512u, xn = (kp1 & ~1792u) | 4; OPQ(kp1); OPQ(xr); OPQ(xn);
  SB();
  b8 f1158 = cat(l1158, h1158);
  MA(cL110, f1158, asb(x15_0)); SB();
  s4 l1161 = TR(pW0, 61440);
  SB();
  MA(cL111, f1158, asb(x15_1)); SB();
  s4 h1161 = TR(pW0, 61952);
  SB();
  unsigned ka1 = *(LDSP unsigned*)xr, ql0 = *(LDSP unsigned*)xn, ql1 = *(LDSP unsigned*)(xn + 1024);
  float gt0 = *(LDSP float*)(xn + 256), gt1 = *(LDSP float*)(xn + 1280);
  SB();
  b8 f1159 = cat(l1159, h1159);
  MA(cL100, f1159, asb(x16_0)); SB();
  s4 l1162 = TR(pW1, 61440);
  SB();
  u3004_0 = __builtin_amdgcn_exp2f(u3004_0); OPQ(u3004_0);
  SB();
  v3004_0 = __builtin_amdgcn_exp2f(v3004_0); OPQ(v3004_0);
  SB();
  MA(cL101, f1159, asb(x16_1)); SB();
  s4 h1162 = TR(pW1, 61952);
  SB();
  u3004_0 += 1.f; OPQ(u3004_0); SB(); v3004_0 += 1.f; OPQ(v3004_0);
  SB();
  b8 f1160 = cat(l1160, h1160);
  MA(cL110, f1160, asb(x16_0)); SB();
  s4 l1163 = TR(pW0, 49152);
  SB();
  { unsigned t_; asm volatile("s_add_u32 m0, %[wb], 131072\n\ts_mov_b32 %[t], 2359680\n\tbuffer_load_dwordx4 %[vW1], %[rW1], %[t] offen lds" : [t]"=&s"(t_) : [wb]"s"(wb), [vW1]"v"(vW1), [rW1]"s"(rW1) : "memory", "scc"); }
  SB();
  MA(cL111, f1160, asb(x16_1)); SB();
  s4 h1163 = TR(pW0, 49664);
  SB();
  u3004_0 = __builtin_amdgcn_rcpf(u3004_0); OPQ(u3004_0);
  SB();
  b8 f1161 = cat(l1161, h1161);
  MF(cL100, f1161, asb(x17_0)); SB();
  s4 l1164 = TR(pW1, 49152);
  SB();
  v3004_0 = __builtin_amdgcn_rcpf(v3004_0); OPQ(v3004_0);
  SB();
  MF(cL101, f1161, asb(x17_1)); SB();
  s4 h1164 = TR(pW1, 49664);
  SB();
  u3004_0 *= cF101[0]; OPQ(u3004_0);
  SB();
  unsigned z3004_0 = pk(u3004_0, v3004_0 * cF101[1]); OPQ(z3004_0);
  SB();
  b8 f1162 = cat(l1162, h1162);
  MF(cL110, f1162, asb(x17_0)); SB();
  s4 l1165 = TR(pW0, 53248);
  SB();
  float u3004_1 = cF101[2] * -1.4426950408889634f; OPQ(u3004_1); SB(); float v3004_1 = cF101[3] * -1.4426950408889634f; OPQ(v3004_1);
  SB();
  MF(cL111, f1162, asb(x17_1)); SB();
  s4 h1165 = TR(pW0, 53760);
  SB();
  u3004_1 = __builtin_amdgcn_exp2f(u3004_1); OPQ(u3004_1);
  SB();
  SB();
  b8 f1163 = cat(l1163, h1163);
  MZ(cL200, f1163, asb(x0_0)); SB();
  s4 l1166 = TR(pW1, 53248);
  SB();
  v3004_1 = __builtin_amdgcn_exp2f(v3004_1); OPQ(v3004_1);
  SB();
  MZ(cL201, f1163, asb(x0_1)); SB();
  s4 h1166 = TR(pW1, 53760);
  SB();
  u3004_1 += 1.f; OPQ(u3004_1); SB(); v3004_1 += 1.f; OPQ(v3004_1);
  SB();
  b8 f1164 = cat(l1164, h1164);
  MZ(cL210, f1164, asb(x0_0)); SB();
  pW0 += 81920u; OPQ(pW0);
  s4 l1167 = TR(pW0, 57344);
  SB();
  u3004_1 = __builtin_amdgcn_rcpf(u3004_1); OPQ(u3004_1);
  SB();
  MZ(cL211, f1164, asb(x0_1)); SB();
  s4 h1167 = TR(pW0, 57856);
  SB();
  v3004_1 = __builtin_amdgcn_rcpf(v3004_1); OPQ(v3004_1);
  SB();
  u3004_1 *= cF101[2]; OPQ(u3004_1);
  SB();
  b8 f1165 = cat(l1165, h1165);
  MA(cL200, f1165, asb(x1_0)); SB();
  pW1 += 81920u; OPQ(pW1);
  s4 l1168 = TR(pW1, 57344);
  SB();
  unsigned kc1 = 0x3f80u; OPQ(kc1); SB(); u4 kf1 = cat2(pk4(cL101), (u2){ka1, kc1}); OPQ(kf1);
  SB();
  MA(cL201, f1165, asb(x1_1)); SB();
  s4 h1168 = TR(pW1, 57856);
  SB();
  st16(kf1, rWS, v16, sKV + (1 ^ cg) * 1024);
  SB();
  LR(kp1, 0) = kf1;
  SB();
  b8 f1166 = cat(l1166, h1166);
  MA(cL210, f1166, asb(x1_0)); SB();
  s4 l1169 = TR(pW0, 61440);
  SB();
  unsigned kc0 = 0x3f80u; OPQ(kc0); SB(); u4 kf0 = cat2(pk4(cL100), (u2){kx[0], kc0}); OPQ(kf0);
  SB();
  MA(cL211, f1166, asb(x1_1)); SB();
  s4 h1169 = TR(pW0, 61952);
  SB();
  st16(kf0, rWS, v16, sKV + (0 ^ cg) * 1024);
  SB();
  b8 f1167 = cat(l1167, h1167);
  MA(cL200, f1167, asb(x2_0)); SB();
  s4 l1170 = TR(pW1, 61440);
  SB();
  LR(kp1 ^ 1024, 0) = kf0;
  SB();
  MA(cL201, f1167, asb(x2_1)); SB();
  s4 h1170 = TR(pW1, 61952);
  SB();
  ql0 = LN(0xffffull) ? ql0 : 0u; OPQ(ql0); SB(); ql1 = LN(0xffffull) ? ql1 : 0u; OPQ(ql1); OPQ(gt0); OPQ(gt1); SB(); unsigned qs00, qs02, qs10, qs12;
  SB();
  { asm volatile("s_waitcnt vmcnt(10)\n\ts_barrier" :  :  : "memory"); }
  SB();
  b8 f1168 = cat(l1168, h1168);
  MA(cL210, f1168, asb(x2_0)); SB();
  s4 l1171 = TR(pW0, 16384);
  SB();
  { unsigned t_; asm volatile("s_add_u32 m0, %[wb], 155648\n\ts_mov_b32 %[t], 1769856\n\tbuffer_load_dwordx4 %[vW1], %[rW1], %[t] offen lds" : [t]"=&s"(t_) : [wb]"s"(wb), [vW1]"v"(vW1), [rW1]"s"(rW1) : "memory", "scc"); }
  SB();
  MA(cL211, f1168, asb(x2_1)); SB();
  s4 h1171 = TR(pW0, 16896);
  SB();
  { float a_ = cL110[0] * 0.34004648f; OPQ(a_); SB(); float b_ = cL110[1] * 0.34004648f; qs00 = pk(a_, b_); OPQ(qs00); }
  SB();
  b8 f1169 = cat(l1169, h1169);
  MA(cL200, f1169, asb(x3_0)); SB();
  s4 l1172 = TR(pW1, 16384);
  SB();
  { float a_ = cL111[0] * 0.34004648f; OPQ(a_); SB(); float b_ = cL111[1] * 0.34004648f; qs10 = pk(a_, b_); OPQ(qs10); }
  SB();
  MA(cL201, f1169, asb(x3_1)); SB();
  s4 h1172 = TR(pW1, 16896);
  SB();
  b8 f1170 = cat(l1170, h1170);
  MA(cL210, f1170, asb(x3_0)); SB();
  s4 l1173 = TR(pW0, 20480);
  SB();
  { float a_ = cL110[2] * 0.34004648f; OPQ(a_); SB(); float b_ = cL110[3] * 0.34004648f; qs02 = pk(a_, b_); OPQ(qs02); }
  SB();
  MA(cL211, f1170, asb(x3_1)); SB();
  s4 h1173 = TR(pW0, 20992);
  SB();
  { float a_ = cL111[2] * 0.34004648f; OPQ(a_); SB(); float b_ = cL111[3] * 0.34004648f; qs12 = pk(a_, b_); OPQ(qs12); }
  SB();
  b8 f1171 = cat(l1171, h1171);
  MA(cL200, f1171, asb(x4_0)); SB();
  s4 l1174 = TR(pW1, 20480);
  SB();
  unsigned qm00 = cg ? qs10 : qs00; OPQ(qm00); SB(); unsigned qm10 = cg ? qs00 : qs10; OPQ(qm10);
  SB();
  MA(cL201, f1171, asb(x4_1)); SB();
  s4 h1174 = TR(pW1, 20992);
  SB();
  unsigned qm02 = cg ? qs12 : qs02; OPQ(qm02); SB(); unsigned qm12 = cg ? qs02 : qs12; OPQ(qm12);
  SB();
  b8 f1172 = cat(l1172, h1172);
  MA(cL210, f1172, asb(x4_0)); SB();
  s4 l1175 = TR(pW0, 40960);
  SB();
  unsigned z3004_1 = pk(u3004_1, v3004_1 * cF101[3]); OPQ(z3004_1);
  SB();
  MA(cL211, f1172, asb(x4_1)); SB();
  s4 h1175 = TR(pW0, 41472);
  SB();
  float u3004_2 = cF111[0] * -1.4426950408889634f; OPQ(u3004_2); SB(); float v3004_2 = cF111[1] * -1.4426950408889634f; OPQ(v3004_2);
  SB();
  b8 f1173 = cat(l1173, h1173);
  MA(cL200, f1173, asb(x5_0)); SB();
  s4 l1176 = TR(pW1, 40960);
  SB();
  u3004_2 = __builtin_amdgcn_exp2f(u3004_2); OPQ(u3004_2);
  SB();
  MA(cL201, f1173, asb(x5_1)); SB();
  s4 h1176 = TR(pW1, 41472);
  SB();
  v3004_2 = __builtin_amdgcn_exp2f(v3004_2); OPQ(v3004_2);
  SB();
  b8 f1174 = cat(l1174, h1174);
  MA(cL210, f1174, asb(x5_0)); SB();
  s4 l1177 = TR(pW0, 45056);
  SB();
  u3004_2 += 1.f; OPQ(u3004_2); SB(); v3004_2 += 1.f; OPQ(v3004_2);
  SB();
  u3004_2 = __builtin_amdgcn_rcpf(u3004_2); OPQ(u3004_2);
  SB();
  MA(cL211, f1174, asb(x5_1)); SB();
  s4 h1177 = TR(pW0, 45568);
  SB();
  v3004_2 = __builtin_amdgcn_rcpf(v3004_2); OPQ(v3004_2);
  SB();
  b8 f1175 = cat(l1175, h1175);
  MA(cL200, f1175, asb(x6_0)); SB();
  s4 l1178 = TR(pW1, 45056);
  SB();
  u3004_2 *= cF111[0]; OPQ(u3004_2);
  SB();
  MA(cL201, f1175, asb(x6_1)); SB();
  s4 h1178 = TR(pW1, 45568);
  SB();
  unsigned z3004_2 = pk(u3004_2, v3004_2 * cF111[1]); OPQ(z3004_2);
  SB();
  b8 f1176 = cat(l1176, h1176);
  MA(cL210, f1176, asb(x6_0)); SB();
  s4 l1179 = TR(pW0, 24576);
  SB();
  float u3004_3 = cF111[2] * -1.4426950408889634f; OPQ(u3004_3); SB(); float v3004_3 = cF111[3] * -1.4426950408889634f; OPQ(v3004_3);
  SB();
  u3004_3 = __builtin_amdgcn_exp2f(u3004_3); OPQ(u3004_3);
  SB();
  MA(cL211, f1176, asb(x6_1)); SB();
  s4 h1179 = TR(pW0, 25088);
  SB();
  v3004_3 = __builtin_amdgcn_exp2f(v3004_3); OPQ(v3004_3);
  SB();
  b8 f1177 = cat(l1177, h1177);
  MA(cL200, f1177, asb(x7_0)); SB();
  s4 l1180 = TR(pW1, 24576);
  SB();
  u3004_3 += 1.f; OPQ(u3004_3); SB(); v3004_3 += 1.f; OPQ(v3004_3);
  SB();
  MA(cL201, f1177, asb(x7_1)); SB();
  s4 h1180 = TR(pW1, 25088);
  SB();
  u3004_3 = __builtin_amdgcn_rcpf(u3004_3); OPQ(u3004_3);
  SB();
  b8 f1178 = cat(l1178, h1178);
  MA(cL210, f1178, asb(x7_0)); SB();
  s4 l1181 = TR(pW0, 28672);
  SB();
  v3004_3 = __builtin_amdgcn_rcpf(v3004_3); OPQ(v3004_3);
  SB();
  u3004_3 *= cF111[2]; OPQ(u3004_3);
  SB();
  MA(cL211, f1178, asb(x7_1)); SB();
  s4 h1181 = TR(pW0, 29184);
  SB();
  unsigned z3004_3 = pk(u3004_3, v3004_3 * cF111[3]); OPQ(z3004_3);
  SB();
  b8 f1179 = cat(l1179, h1179);
  MA(cL200, f1179, asb(x8_0)); SB();
  s4 l1182 = TR(pW1, 28672);
  SB();
  u4 hk11 = (u4){z3004_0, z3004_1, z3004_2, z3004_3}; OPQ(hk11);
  st16(hk11, rWS, v16, sH + (mF + 1) * 16384 + cg * 8192 + 1024);
  SB();
  MA(cL201, f1179, asb(x8_1)); SB();
  s4 h1182 = TR(pW1, 29184);
  SB();
  float u3007_0 = cH0[0] * -1.4426950408889634f; OPQ(u3007_0); SB(); float v3007_0 = cH0[1] * -1.4426950408889634f; OPQ(v3007_0);
  SB();
  b8 f1180 = cat(l1180, h1180);
  MA(cL210, f1180, asb(x8_0)); SB();
  s4 l1183 = TR(pW0, 8192);
  SB();
  { asm volatile("s_waitcnt vmcnt(2)" :  :  : "memory"); }
  st8((u2){tlo, thi}, rWS, vF + 0, sGK); SB();
  SB();
  MA(cL211, f1180, asb(x8_1)); SB();
  s4 h1183 = TR(pW0, 8704);
  SB();
  u3007_0 = __builtin_amdgcn_exp2f(u3007_0); OPQ(u3007_0);
  SB();
  v3007_0 = __builtin_amdgcn_exp2f(v3007_0); OPQ(v3007_0);
  SB();
  b8 f1181 = cat(l1181, h1181);
  MA(cL200, f1181, asb(x9_0)); SB();
  s4 l1184 = TR(pW1, 8192);
  SB();
  const unsigned o2 = gi * 1152u * 1152u;
  unsigned sX0 = (j >> 1) * 663552u, sX2 = sX0 ^ 663552u, sX1 = (j & 1) * 368640u, sX3 = (sX1 ^ 368640u) + sX2; sX2 += sX1; sX0 += sX1; sX1 = 1032192u - sX2; OPS(sX0); OPS(sX1); OPS(sX2); OPS(sX3);
  unsigned lx_ = lnX; asm volatile("" : "+s"(lx_)); const rsrc_t rW2X = mk(pw2 + o2 + lx_ * 128, 1152u * 1152u - lx_ * 128);
  SB();
  MA(cL201, f1181, asb(x9_1)); SB();
  s4 h1184 = TR(pW1, 8704);
  SB();
  const unsigned sFX = sFW + 4096;
  u2 pW2;
  { asm volatile("buffer_load_dwordx2 %[pW2], %[v16], %[rWS], %[sFX] offen offset:0 sc1" : [pW2]"=&v"(pW2) : [v16]"v"(v16), [rWS]"s"(rWS), [sFX]"s"(sFX) : "memory"); }
  SB();
  b8 f1182 = cat(l1182, h1182);
  MA(cL210, f1182, asb(x9_0)); SB();
  s4 l1185 = TR(pW0, 12288);
  SB();
  u3007_0 += 1.f; OPQ(u3007_0); SB(); v3007_0 += 1.f; OPQ(v3007_0);
  SB();
  MA(cL211, f1182, asb(x9_1)); SB();
  s4 h1185 = TR(pW0, 12800);
  SB();
  u3007_0 = __builtin_amdgcn_rcpf(u3007_0); OPQ(u3007_0);
  SB();
  b8 f1183 = cat(l1183, h1183);
  MA(cL200, f1183, asb(x10_0)); SB();
  s4 l1186 = TR(pW1, 12288);
  SB();
  v3007_0 = __builtin_amdgcn_rcpf(v3007_0); OPQ(v3007_0);
  SB();
  u3007_0 *= cH0[0]; OPQ(u3007_0);
  SB();
  MA(cL201, f1183, asb(x10_1)); SB();
  s4 h1186 = TR(pW1, 12800);
  SB();
  unsigned z3007_0 = pk(u3007_0, v3007_0 * cH0[1]); OPQ(z3007_0);
  SB();
  b8 f1184 = cat(l1184, h1184);
  MA(cL210, f1184, asb(x10_0)); SB();
  s4 l1187 = TR(pW0, 0);
  SB();
  float u3007_1 = cH0[2] * -1.4426950408889634f; OPQ(u3007_1); SB(); float v3007_1 = cH0[3] * -1.4426950408889634f; OPQ(v3007_1);
  SB();
  MA(cL211, f1184, asb(x10_1)); SB();
  s4 h1187 = TR(pW0, 512);
  SB();
  u3007_1 = __builtin_amdgcn_exp2f(u3007_1); OPQ(u3007_1);
  SB();
  b8 f1185 = cat(l1185, h1185);
  MA(cL200, f1185, asb(x11_0)); SB();
  s4 l1188 = TR(pW1, 0);
  SB();
  v3007_1 = __builtin_amdgcn_exp2f(v3007_1); OPQ(v3007_1);
  SB();
  MA(cL201, f1185, asb(x11_1)); SB();
  s4 h1188 = TR(pW1, 512);
  SB();
  u3007_1 += 1.f; OPQ(u3007_1); SB(); v3007_1 += 1.f; OPQ(v3007_1);
  SB();
  b8 f1186 = cat(l1186, h1186);
  MA(cL210, f1186, asb(x11_0)); SB();
  s4 l1189 = TR(pW0, 4096);
  SB();
  u3007_1 = __builtin_amdgcn_rcpf(u3007_1); OPQ(u3007_1);
  SB();
  MA(cL211, f1186, asb(x11_1)); SB();
  s4 h1189 = TR(pW0, 4608);
  SB();
  v3007_1 = __builtin_amdgcn_rcpf(v3007_1); OPQ(v3007_1);
  SB();
  u3007_1 *= cH0[2]; OPQ(u3007_1);
  SB();
  b8 f1187 = cat(l1187, h1187);
  MA(cL200, f1187, asb(x12_0)); SB();
  s4 l1190 = TR(pW1, 4096);
  SB();
  unsigned z3007_1 = pk(u3007_1, v3007_1 * cH0[3]); OPQ(z3007_1);
  SB();
  MA(cL201, f1187, asb(x12_1)); SB();
  s4 h1190 = TR(pW1, 4608);
  SB();
  float u3007_2 = cH1[0] * -1.4426950408889634f; OPQ(u3007_2); SB(); float v3007_2 = cH1[1] * -1.4426950408889634f; OPQ(v3007_2);
  SB();
  { asm volatile("s_waitcnt vmcnt(7)\n\ts_barrier" :  :  : "memory"); }
  SB();
  b8 f1188 = cat(l1188, h1188);
  MA(cL210, f1188, asb(x12_0)); SB();
  pW0 += 4294901760u; OPQ(pW0);
  s4 l1191 = TR(pW0, 57344);
  SB();
  { unsigned t_; asm volatile("s_add_u32 m0, %[wb], 106496\n\ts_mov_b32 %[t], 2949504\n\tbuffer_load_dwordx4 %[vW1], %[rW1], %[t] offen lds" : [t]"=&s"(t_) : [wb]"s"(wb), [vW1]"v"(vW1), [rW1]"s"(rW1) : "memory", "scc"); }
  SB();
  MA(cL211, f1188, asb(x12_1)); SB();
  s4 h1191 = TR(pW0, 57856);
  SB();
  u3007_2 = __builtin_amdgcn_exp2f(u3007_2); OPQ(u3007_2);
  SB();
  b8 f1189 = cat(l1189, h1189);
  MA(cL200, f1189, asb(x13_0)); SB();
  pW1 += 4294901760u; OPQ(pW1);
  s4 l1192 = TR(pW1, 57344);
  SB();
  v3007_2 = __builtin_amdgcn_exp2f(v3007_2); OPQ(v3007_2);
  SB();
  u3007_2 += 1.f; OPQ(u3007_2); SB(); v3007_2 += 1.f; OPQ(v3007_2);
  SB();
  MA(cL201, f1189, asb(x13_1)); SB();
  s4 h1192 = TR(pW1, 57856);
  SB();
  u3007_2 = __builtin_amdgcn_rcpf(u3007_2); OPQ(u3007_2);
  SB();
  b8 f1190 = cat(l1190, h1190);
  MA(cL210, f1190, asb(x13_0)); SB();
  s4 l1193 = TR(pW0, 61440);
  SB();
  v3007_2 = __builtin_amdgcn_rcpf(v3007_2); OPQ(v3007_2);
  SB();
  MA(cL211, f1190, asb(x13_1)); SB();
  s4 h1193 = TR(pW0, 61952);
  SB();
  u3007_2 *= cH1[0]; OPQ(u3007_2);
  SB();
  b8 f1191 = cat(l1191, h1191);
  MA(cL200, f1191, asb(x14_0)); SB();
  s4 l1194 = TR(pW1, 61440);
  SB();
  { unsigned t_; asm volatile("s_add_u32 m0, %[wb], 98304\n\ts_mov_b32 %[t], 3539328\n\tbuffer_load_dwordx4 %[vW1], %[rW1], %[t] offen lds" : [t]"=&s"(t_) : [wb]"s"(wb), [vW1]"v"(vW1), [rW1]"s"(rW1) : "memory", "scc"); }
  SB();
  MA(cL201, f1191, asb(x14_1)); SB();
  s4 h1194 = TR(pW1, 61952);
  SB();
  { asm volatile("s_waitcnt vmcnt(2)" : [pW2]"+v"(pW2) :  : "memory"); }
  repoll<0>(pW2, rWS, v16, sFX, T_); SB();
  SB();
  unsigned z3007_2 = pk(u3007_2, v3007_2 * cH1[1]); OPQ(z3007_2);
  SB();
  b8 f1192 = cat(l1192, h1192);
  MA(cL210, f1192, asb(x14_0)); SB();
  s4 l1195 = TR(pW0, 8192);
  SB();
  float u3007_3 = cH1[2] * -1.4426950408889634f; OPQ(u3007_3); SB(); float v3007_3 = cH1[3] * -1.4426950408889634f; OPQ(v3007_3);
  SB();
  MA(cL211, f1192, asb(x14_1)); SB();
  s4 h1195 = TR(pW0, 8704);
  SB();
  u3007_3 = __builtin_amdgcn_exp2f(u3007_3); OPQ(u3007_3);
  SB();
  b8 f1193 = cat(l1193, h1193);
  MA(cL200, f1193, asb(x15_0)); SB();
  s4 l1196 = TR(pW1, 8192);
  SB();
  unsigned v1 = v16 & 16; OPQ(v1);
  u2 pV;
  { asm volatile("buffer_load_dwordx2 %[pV], %[v1], %[rWS], %[sFK] offen offset:256 sc1" : [pV]"=&v"(pV) : [v1]"v"(v1), [rWS]"s"(rWS), [sFK]"s"(sFK) : "memory"); }
  SB();
  MA(cL201, f1193, asb(x15_1)); SB();
  s4 h1196 = TR(pW1, 8704);
  SB();
  v3007_3 = __builtin_amdgcn_exp2f(v3007_3); OPQ(v3007_3);
  SB();
  b8 f1194 = cat(l1194, h1194);
  MA(cL210, f1194, asb(x15_0)); SB();
  s4 l1197 = TR(pW0, 12288);
  SB();
  { unsigned t_; asm volatile("s_add_u32 m0, %[wb], 122880\n\ts_mov_b32 %[t], 4129152\n\tbuffer_load_dwordx4 %[vW1], %[rW1], %[t] offen lds" : [t]"=&s"(t_) : [wb]"s"(wb), [vW1]"v"(vW1), [rW1]"s"(rW1) : "memory", "scc"); }
  SB();
  MA(cL211, f1194, asb(x15_1)); SB();
  s4 h1197 = TR(pW0, 12800);
  SB();
  u3007_3 += 1.f; OPQ(u3007_3); SB(); v3007_3 += 1.f; OPQ(v3007_3);
  SB();
  b8 f1195 = cat(l1195, h1195);
  MA(cL200, f1195, asb(x16_0)); SB();
  s4 l1198 = TR(pW1, 12288);
  SB();
  u3007_3 = __builtin_amdgcn_rcpf(u3007_3); OPQ(u3007_3);
  SB();
  MA(cL201, f1195, asb(x16_1)); SB();
  s4 h1198 = TR(pW1, 12800);
  SB();
  v3007_3 = __builtin_amdgcn_rcpf(v3007_3); OPQ(v3007_3);
  SB();
  u3007_3 *= cH1[2]; OPQ(u3007_3);
  SB();
  b8 f1196 = cat(l1196, h1196);
  MA(cL210, f1196, asb(x16_0)); SB();
  s4 l1199 = TR(pW0, 0);
  SB();
  unsigned z3007_3 = pk(u3007_3, v3007_3 * cH1[3]); OPQ(z3007_3);
  SB();
  MA(cL211, f1196, asb(x16_1)); SB();
  s4 h1199 = TR(pW0, 512);
  SB();
  st16(((j ^ cg) & 1) ? (u4){z3007_2, z3007_3, z3007_0, z3007_1} : (u4){z3007_0, z3007_1, z3007_2, z3007_3}, rWS, v16, sH + (16 + (j >> 1)) * 16384 + (j & 1) * 8192 + cg * 1024);
  SB();
  b8 f1197 = cat(l1197, h1197);
  MF(cL200, f1197, asb(x17_0)); SB();
  s4 l1200 = TR(pW1, 0);
  SB();
  { unsigned t_; asm volatile("s_add_u32 m0, %[wb], 139264\n\ts_mov_b32 %[t], 4718976\n\tbuffer_load_dwordx4 %[vW1], %[rW1], %[t] offen lds" : [t]"=&s"(t_) : [wb]"s"(wb), [vW1]"v"(vW1), [rW1]"s"(rW1) : "memory", "scc"); }
  SB();
  MF(cL201, f1197, asb(x17_1)); SB();
  s4 h1200 = TR(pW1, 512);
  SB();
  b8 f1198 = cat(l1198, h1198);
  MF(cL210, f1198, asb(x17_0)); SB();
  s4 l1201 = TR(pW0, 4096);
  SB();
  MF(cL211, f1198, asb(x17_1)); SB();
  s4 h1201 = TR(pW0, 4608);
  SB();
  SB();
  b8 f1199 = cat(l1199, h1199);
  MZ(cL300, f1199, asb(x0_0)); SB();
  s4 l1202 = TR(pW1, 4096);
  SB();
  MZ(cL301, f1199, asb(x0_1)); SB();
  s4 h1202 = TR(pW1, 4608);
  SB();
  b8 f1200 = cat(l1200, h1200);
  MZ(cL310, f1200, asb(x0_0)); SB();
  pW0 += 81920u; OPQ(pW0);
  s4 l1203 = TR(pW0, 16384);
  SB();
  { unsigned t_; asm volatile("s_add_u32 m0, %[wb], 90112\n\ts_add_u32 %[t], %[sX2], 0\n\tbuffer_load_dwordx4 %[vW2], %[rW2X], %[t] offen lds" : [t]"=&s"(t_) : [wb]"s"(wb), [sX2]"s"(sX2), [vW2]"v"(vW2), [rW2X]"s"(rW2X) : "memory", "scc"); }
  SB();
  MZ(cL311, f1200, asb(x0_1)); SB();
  s4 h1203 = TR(pW0, 16896);
  SB();
  b8 f1201 = cat(l1201, h1201);
  MA(cL300, f1201, asb(x1_0)); SB();
  pW1 += 81920u; OPQ(pW1);
  s4 l1204 = TR(pW1, 16384);
  SB();
  { asm volatile("s_waitcnt vmcnt(4)" : [pV]"+v"(pV) :  : "memory"); }
  repoll<256>(pV, rWS, v1, sFK, T_); SB();
  SB();
  MA(cL301, f1201, asb(x1_1)); SB();
  s4 h1204 = TR(pW1, 16896);
  SB();
  b8 f1202 = cat(l1202, h1202);
  MA(cL310, f1202, asb(x1_0)); SB();
  s4 l1205 = TR(pW0, 20480);
  SB();
  MA(cL311, f1202, asb(x1_1)); SB();
  s4 h1205 = TR(pW0, 20992);
  SB();
  b8 f1203 = cat(l1203, h1203);
  MA(cL300, f1203, asb(x2_0)); SB();
  s4 l1206 = TR(pW1, 20480);
  SB();
  const unsigned sk0 = Rb >= 2 ? sKV - 2 * 393216 : FAR, sk1 = Rb >= 1 ? sKV - 393216 : FAR;
  { const unsigned kvb = lb + 49152 - 2048 + (r * 2 + cg) * 1024 + 0;
  { asm volatile("s_mov_b32 m0, %[kvb]\n\ts_nop 0\n\tbuffer_load_dwordx4 %[v16], %[rWS], %[sk0] offen offset:2048 sc1 lds" :  : [kvb]"s"(kvb), [v16]"v"(v16), [rWS]"s"(rWS), [sk0]"s"(sk0) : "memory"); }
  }
  SB();
  float u3005_0 = cL200[0] * -1.4426950408889634f; OPQ(u3005_0); SB(); float v3005_0 = cL200[1] * -1.4426950408889634f; OPQ(v3005_0);
  SB();
  MA(cL301, f1203, asb(x2_1)); SB();
  s4 h1206 = TR(pW1, 20992);
  SB();
  u3005_0 = __builtin_amdgcn_exp2f(u3005_0); OPQ(u3005_0);
  SB();
  b8 f1204 = cat(l1204, h1204);
  MA(cL310, f1204, asb(x2_0)); SB();
  s4 l1207 = TR(pW0, 49152);
  SB();
  MA(cL311, f1204, asb(x2_1)); SB();
  s4 h1207 = TR(pW0, 49664);
  SB();
  b8 f1205 = cat(l1205, h1205);
  MA(cL300, f1205, asb(x3_0)); SB();
  s4 l1208 = TR(pW1, 49152);
  SB();
  MA(cL301, f1205, asb(x3_1)); SB();
  s4 h1208 = TR(pW1, 49664);
  SB();
  b8 f1206 = cat(l1206, h1206);
  MA(cL310, f1206, asb(x3_0)); SB();
  s4 l1209 = TR(pW0, 53248);
  SB();
  { const unsigned kvb = lb + 49152 - 2048 + (r * 2 + cg) * 1024 + 8192;
  { asm volatile("s_mov_b32 m0, %[kvb]\n\ts_nop 0\n\tbuffer_load_dwordx4 %[v16], %[rWS], %[sk1] offen offset:2048 sc1 lds" :  : [kvb]"s"(kvb), [v16]"v"(v16), [rWS]"s"(rWS), [sk1]"s"(sk1) : "memory"); }
  }
  SB();
  u2 pK;
  { asm volatile("buffer_load_dwordx2 %[pK], %[v1], %[rWS], %[sFK] offen offset:0 sc1" : [pK]"=&v"(pK) : [v1]"v"(v1), [rWS]"s"(rWS), [sFK]"s"(sFK) : "memory"); }
  SB();
  MA(cL311, f1206, asb(x3_1)); SB();
  s4 h1209 = TR(pW0, 53760);
  SB();
  v3005_0 = __builtin_amdgcn_exp2f(v3005_0); OPQ(v3005_0);
  SB();
  b8 f1207 = cat(l1207, h1207);
  MA(cL300, f1207, asb(x4_0)); SB();
  s4 l1210 = TR(pW1, 53248);
  SB();
  const rsrc_t rH = mk(pws + sH + cg * 8192, 18u * 16384u); const float NEG = -__builtin_inff(); const unsigned sJ = j << 16;
  SB();
  unsigned pA1 = pA0 ^ 32, pA2 = pA0 ^ 64, pA3 = pA0 ^ 96; OPQ(pA1); OPQ(pA2); OPQ(pA3);
  SB();
  MA(cL301, f1207, asb(x4_1)); SB();
  s4 h1210 = TR(pW1, 53760);
  SB();
  unsigned pK_ = lb + (r * 2 + cg) * 2048 + v16; OPQ(pK_); SB(); unsigned one0 = 0x3f800000u, one1 = 0x3f800000u; OPQ(one0); OPQ(one1);
  const b8 qf0 = asb((u4){qm00, qm02, ql0, one0}), qf1 = asb((u4){qm10, qm12, ql1, one1}); f4 cd0;
  SB();
  { asm volatile("s_waitcnt vmcnt(6)\n\ts_barrier" :  :  : "memory"); }
  SB();
  b8 f1208 = cat(l1208, h1208);
  MA(cL310, f1208, asb(x4_0)); SB();
  s4 l1211 = TR(pW0, 57344);
  SB();
  cd0[0] = LN(0xfff00ff000f0000ull) ? 0.f : NEG; cd0[1] = LN(0x1fff01ff001f0001ull) ? 0.f : NEG;
  SB();
  MA(cL311, f1208, asb(x4_1)); SB();
  s4 h1211 = TR(pW0, 57856);
  SB();
  cd0[2] = LN(0x3fff03ff003f0003ull) ? 0.f : NEG; cd0[3] = LN(0x7fff07ff007f0007ull) ? 0.f : NEG; OPQ(cd0);
  SB();
  b8 f1209 = cat(l1209, h1209);
  MA(cL300, f1209, asb(x5_0)); SB();
  s4 l1212 = TR(pW1, 57344);
  SB();
  { unsigned t_; asm volatile("s_add_u32 m0, %[wb], 114688\n\ts_add_u32 %[t], %[sX0], 0\n\tbuffer_load_dwordx4 %[vW2], %[rW2X], %[t] offen lds" : [t]"=&s"(t_) : [wb]"s"(wb), [sX0]"s"(sX0), [vW2]"v"(vW2), [rW2X]"s"(rW2X) : "memory", "scc"); }
  SB();
  MA(cL301, f1209, asb(x5_1)); SB();
  s4 h1212 = TR(pW1, 57856);
  SB();
  u3005_0 += 1.f; OPQ(u3005_0); SB(); v3005_0 += 1.f; OPQ(v3005_0);
  SB();
  b8 f1210 = cat(l1210, h1210);
  MA(cL310, f1210, asb(x5_0)); SB();
  s4 l1213 = TR(pW0, 61440);
  SB();
  { asm volatile("s_waitcnt vmcnt(6)" :  :  : "memory"); }
  st8((u2){tlo, thi}, rWS, vF + 0, sGH); SB();
  SB();
  MA(cL311, f1210, asb(x5_1)); SB();
  s4 h1213 = TR(pW0, 61952);
  SB();
  u3005_0 = __builtin_amdgcn_rcpf(u3005_0); OPQ(u3005_0);
  SB();
  b8 f1211 = cat(l1211, h1211);
  MA(cL300, f1211, asb(x6_0)); SB();
  s4 l1214 = TR(pW1, 61440);
  SB();
  v3005_0 = __builtin_amdgcn_rcpf(v3005_0); OPQ(v3005_0);
  SB();
  u3005_0 *= cL200[0]; OPQ(u3005_0);
  SB();
  MA(cL301, f1211, asb(x6_1)); SB();
  s4 h1214 = TR(pW1, 61952);
  SB();
  { unsigned t_; asm volatile("s_add_u32 m0, %[wb], 73728\n\ts_add_u32 %[t], %[sX1], 0\n\tbuffer_load_dwordx4 %[vW2], %[rW2X], %[t] offen lds" : [t]"=&s"(t_) : [wb]"s"(wb), [sX1]"s"(sX1), [vW2]"v"(vW2), [rW2X]"s"(rW2X) : "memory", "scc"); }
  SB();
  b8 f1212 = cat(l1212, h1212);
  MA(cL310, f1212, asb(x6_0)); SB();
  s4 l1215 = TR(pW0, 32768);
  SB();
  unsigned z3005_0 = pk(u3005_0, v3005_0 * cL200[1]); OPQ(z3005_0);
  SB();
  MA(cL311, f1212, asb(x6_1)); SB();
  s4 h1215 = TR(pW0, 33280);
  SB();
  float u3005_1 = cL200[2] * -1.4426950408889634f; OPQ(u3005_1); SB(); float v3005_1 = cL200[3] * -1.4426950408889634f; OPQ(v3005_1);
  SB();
  u3005_1 = __builtin_amdgcn_exp2f(u3005_1); OPQ(u3005_1);
  SB();
  b8 f1213 = cat(l1213, h1213);
  MA(cL300, f1213, asb(x7_0)); SB();
  s4 l1216 = TR(pW1, 32768);
  SB();
  { asm volatile("s_waitcnt vmcnt(3)" : [pK]"+v"(pK) :  : "memory"); }
  repoll<0>(pK, rWS, v1, sFK, T_); SB();
  SB();
  MA(cL301, f1213, asb(x7_1)); SB();
  s4 h1216 = TR(pW1, 33280);
  SB();
  b8 f1214 = cat(l1214, h1214);
  MA(cL310, f1214, asb(x7_0)); SB();
  s4 l1217 = TR(pW0, 36864);
  SB();
  { const unsigned kvb = lb + (r * 2 + cg) * 2048 + 0;
  { asm volatile("s_mov_b32 m0, %[kvb]\n\ts_nop 0\n\tbuffer_load_dwordx4 %[v16], %[rWS], %[sk0] offen sc1 lds\n\tbuffer_load_dwordx4 %[v16], %[rWS], %[sk0] offen offset:1024 sc1 lds" :  : [kvb]"s"(kvb), [v16]"v"(v16), [rWS]"s"(rWS), [sk0]"s"(sk0) : "memory"); }
  }
  SB();
  v3005_1 = __builtin_amdgcn_exp2f(v3005_1); OPQ(v3005_1);
  SB();
  MA(cL311, f1214, asb(x7_1)); SB();
  s4 h1217 = TR(pW0, 37376);
  SB();
  u3005_1 += 1.f; OPQ(u3005_1); SB(); v3005_1 += 1.f; OPQ(v3005_1);
  SB();
  b8 f1215 = cat(l1215, h1215);
  MA(cL300, f1215, asb(x8_0)); SB();
  s4 l1218 = TR(pW1, 36864);
  SB();
  u3005_1 = __builtin_amdgcn_rcpf(u3005_1); OPQ(u3005_1);
  SB();
  v3005_1 = __builtin_amdgcn_rcpf(v3005_1); OPQ(v3005_1);
  SB();
  MA(cL301, f1215, asb(x8_1)); SB();
  s4 h1218 = TR(pW1, 37376);
  SB();
  u3005_1 *= cL200[2]; OPQ(u3005_1);
  SB();
  b8 f1216 = cat(l1216, h1216);
  MA(cL310, f1216, asb(x8_0)); SB();
  s4 l1219 = TR(pW0, 8192);
  SB();
  unsigned z3005_1 = pk(u3005_1, v3005_1 * cL200[3]); OPQ(z3005_1);
  SB();
  float u3005_2 = cL210[0] * -1.4426950408889634f; OPQ(u3005_2); SB(); float v3005_2 = cL210[1] * -1.4426950408889634f; OPQ(v3005_2);
  SB();
  MA(cL311, f1216, asb(x8_1)); SB();
  s4 h1219 = TR(pW0, 8704);
  SB();
  u3005_2 = __builtin_amdgcn_exp2f(u3005_2); OPQ(u3005_2);
  SB();
  b8 f1217 = cat(l1217, h1217);
  MA(cL300, f1217, asb(x9_0)); SB();
  s4 l1220 = TR(pW1, 8192);
  SB();
  v3005_2 = __builtin_amdgcn_exp2f(v3005_2); OPQ(v3005_2);
  SB();
  MA(cL301, f1217, asb(x9_1)); SB();
  s4 h1220 = TR(pW1, 8704);
  SB();
  u3005_2 += 1.f; OPQ(u3005_2); SB(); v3005_2 += 1.f; OPQ(v3005_2);
  SB();
  b8 f1218 = cat(l1218, h1218);
  MA(cL310, f1218, asb(x9_0)); SB();
  s4 l1221 = TR(pW0, 12288);
  SB();
  MA(cL311, f1218, asb(x9_1)); SB();
  s4 h1221 = TR(pW0, 12800);
  SB();
  b8 f1219 = cat(l1219, h1219);
  MA(cL300, f1219, asb(x10_0)); SB();
  s4 l1222 = TR(pW1, 12288);
  SB();
  { const unsigned kvb = lb + (r * 2 + cg) * 2048 + 16384;
  { asm volatile("s_mov_b32 m0, %[kvb]\n\ts_nop 0\n\tbuffer_load_dwordx4 %[v16], %[rWS], %[sk1] offen sc1 lds\n\tbuffer_load_dwordx4 %[v16], %[rWS], %[sk1] offen offset:1024 sc1 lds" :  : [kvb]"s"(kvb), [v16]"v"(v16), [rWS]"s"(rWS), [sk1]"s"(sk1) : "memory"); }
  }
  SB();
  u3005_2 = __builtin_amdgcn_rcpf(u3005_2); OPQ(u3005_2);
  SB();
  MA(cL301, f1219, asb(x10_1)); SB();
  s4 h1222 = TR(pW1, 12800);
  SB();
  v3005_2 = __builtin_amdgcn_rcpf(v3005_2); OPQ(v3005_2);
  SB();
  u3005_2 *= cL210[0]; OPQ(u3005_2);
  SB();
  b8 f1220 = cat(l1220, h1220);
  MA(cL310, f1220, asb(x10_0)); SB();
  s4 l1223 = TR(pW0, 0);
  SB();
  unsigned z3005_2 = pk(u3005_2, v3005_2 * cL210[1]); OPQ(z3005_2);
  SB();
  MA(cL311, f1220, asb(x10_1)); SB();
  s4 h1223 = TR(pW0, 512);
  SB();
  float u3005_3 = cL210[2] * -1.4426950408889634f; OPQ(u3005_3); SB(); float v3005_3 = cL210[3] * -1.4426950408889634f; OPQ(v3005_3);
  SB();
  b8 f1221 = cat(l1221, h1221);
  MA(cL300, f1221, asb(x11_0)); SB();
  s4 l1224 = TR(pW1, 0);
  SB();
  u3005_3 = __builtin_amdgcn_exp2f(u3005_3); OPQ(u3005_3);
  SB();
  v3005_3 = __builtin_amdgcn_exp2f(v3005_3); OPQ(v3005_3);
  SB();
  MA(cL301, f1221, asb(x11_1)); SB();
  s4 h1224 = TR(pW1, 512);
  SB();
  u3005_3 += 1.f; OPQ(u3005_3); SB(); v3005_3 += 1.f; OPQ(v3005_3);
  SB();
  b8 f1222 = cat(l1222, h1222);
  MA(cL310, f1222, asb(x11_0)); SB();
  s4 l1225 = TR(pW0, 4096);
  SB();
  u3005_3 = __builtin_amdgcn_rcpf(u3005_3); OPQ(u3005_3);
  SB();
  MA(cL311, f1222, asb(x11_1)); SB();
  s4 h1225 = TR(pW0, 4608);
  SB();
  v3005_3 = __builtin_amdgcn_rcpf(v3005_3); OPQ(v3005_3);
  SB();
  u3005_3 *= cL210[2]; OPQ(u3005_3);
  SB();
  b8 f1223 = cat(l1223, h1223);
  MA(cL300, f1223, asb(x12_0)); SB();
  s4 l1226 = TR(pW1, 4096);
  SB();
  unsigned z3005_3 = pk(u3005_3, v3005_3 * cL210[3]); OPQ(z3005_3);
  SB();
  u4 hk20 = (u4){z3005_0, z3005_1, z3005_2, z3005_3}; OPQ(hk20);
  st16(hk20, rWS, v16, sH + (mF + 2) * 16384 + cg * 8192 + 0);
  SB();
  MA(cL301, f1223, asb(x12_1)); SB();
  s4 h1226 = TR(pW1, 4608);
  SB();
  float u3006_0 = cL201[0] * -1.4426950408889634f; OPQ(u3006_0); SB(); float v3006_0 = cL201[1] * -1.4426950408889634f; OPQ(v3006_0);
  SB();
  b8 f1224 = cat(l1224, h1224);
  MA(cL310, f1224, asb(x12_0)); SB();
  s4 l1227 = TR(pW0, 24576);
  SB();
  { unsigned t_; asm volatile("s_add_u32 m0, %[wb], 81920\n\ts_add_u32 %[t], %[sX3], 0\n\tbuffer_load_dwordx4 %[vW2], %[rW2X], %[t] offen lds" : [t]"=&s"(t_) : [wb]"s"(wb), [sX3]"s"(sX3), [vW2]"v"(vW2), [rW2X]"s"(rW2X) : "memory", "scc"); }
  SB();
  MA(cL311, f1224, asb(x12_1)); SB();
  s4 h1227 = TR(pW0, 25088);
  SB();
  u3006_0 = __builtin_amdgcn_exp2f(u3006_0); OPQ(u3006_0);
  SB();
  b8 f1225 = cat(l1225, h1225);
  MA(cL300, f1225, asb(x13_0)); SB();
  s4 l1228 = TR(pW1, 24576);
  SB();
  v3006_0 = __builtin_amdgcn_exp2f(v3006_0); OPQ(v3006_0);
  SB();
  MA(cL301, f1225, asb(x13_1)); SB();
  s4 h1228 = TR(pW1, 25088);
  SB();
  u3006_0 += 1.f; OPQ(u3006_0); SB(); v3006_0 += 1.f; OPQ(v3006_0);
  SB();
  u3006_0 = __builtin_amdgcn_rcpf(u3006_0); OPQ(u3006_0);
  SB();
  b8 f1226 = cat(l1226, h1226);
  MA(cL310, f1226, asb(x13_0)); SB();
  s4 l1229 = TR(pW0, 28672);
  SB();
  v3006_0 = __builtin_amdgcn_rcpf(v3006_0); OPQ(v3006_0);
  SB();
  MA(cL311, f1226, asb(x13_1)); SB();
  s4 h1229 = TR(pW0, 29184);
  SB();
  u3006_0 *= cL201[0]; OPQ(u3006_0);
  SB();
  unsigned z3006_0 = pk(u3006_0, v3006_0 * cL201[1]); OPQ(z3006_0);
  SB();
  b8 f1227 = cat(l1227, h1227);
  MA(cL300, f1227, asb(x14_0)); SB();
  s4 l1230 = TR(pW1, 28672);
  SB();
  float u3006_1 = cL201[2] * -1.4426950408889634f; OPQ(u3006_1); SB(); float v3006_1 = cL201[3] * -1.4426950408889634f; OPQ(v3006_1);
  SB();
  MA(cL301, f1227, asb(x14_1)); SB();
  s4 h1230 = TR(pW1, 29184);
  SB();
  u3006_1 = __builtin_amdgcn_exp2f(u3006_1); OPQ(u3006_1);
  SB();
  { asm volatile("s_waitcnt vmcnt(13)\n\ts_barrier" :  :  : "memory"); }
  SB();
  b8 f1228 = cat(l1228, h1228);
  MA(cL310, f1228, asb(x14_0)); SB();
  s4 l1231 = TR(pW0, 40960);
  SB();
  { unsigned t_; asm volatile("s_add_u32 m0, %[wb], 98304\n\ts_add_u32 %[t], %[sX0], 73728\n\tbuffer_load_dwordx4 %[vW2], %[rW2X], %[t] offen lds" : [t]"=&s"(t_) : [wb]"s"(wb), [sX0]"s"(sX0), [vW2]"v"(vW2), [rW2X]"s"(rW2X) : "memory", "scc"); }
  SB();
  MA(cL311, f1228, asb(x14_1)); SB();
  s4 h1231 = TR(pW0, 41472);
  SB();
  v3006_1 = __builtin_amdgcn_exp2f(v3006_1); OPQ(v3006_1);
  SB();
  b8 f1229 = cat(l1229, h1229);
  MA(cL300, f1229, asb(x15_0)); SB();
  s4 l1232 = TR(pW1, 40960);
  SB();
  u3006_1 += 1.f; OPQ(u3006_1); SB(); v3006_1 += 1.f; OPQ(v3006_1);
  SB();
  u3006_1 = __builtin_amdgcn_rcpf(u3006_1); OPQ(u3006_1);
  SB();
  MA(cL301, f1229, asb(x15_1)); SB();
  s4 h1232 = TR(pW1, 41472);
  SB();
  v3006_1 = __builtin_amdgcn_rcpf(v3006_1); OPQ(v3006_1);
  SB();
  b8 f1230 = cat(l1230, h1230);
  MA(cL310, f1230, asb(x15_0)); SB();
  s4 l1233 = TR(pW0, 45056);
  SB();
  u3006_1 *= cL201[2]; OPQ(u3006_1);
  SB();
  unsigned z3006_1 = pk(u3006_1, v3006_1 * cL201[3]); OPQ(z3006_1);
  SB();
  MA(cL311, f1230, asb(x15_1)); SB();
  s4 h1233 = TR(pW0, 45568);
  SB();
  { unsigned t_; asm volatile("s_add_u32 m0, %[wb], 106496\n\ts_add_u32 %[t], %[sX1], 73728\n\tbuffer_load_dwordx4 %[vW2], %[rW2X], %[t] offen lds" : [t]"=&s"(t_) : [wb]"s"(wb), [sX1]"s"(sX1), [vW2]"v"(vW2), [rW2X]"s"(rW2X) : "memory", "scc"); }
  SB();
  b8 f1231 = cat(l1231, h1231);
  MA(cL300, f1231, asb(x16_0)); SB();
  s4 l1234 = TR(pW1, 45056);
  SB();
  float u3006_2 = cL211[0] * -1.4426950408889634f; OPQ(u3006_2); SB(); float v3006_2 = cL211[1] * -1.4426950408889634f; OPQ(v3006_2);
  SB();
  MA(cL301, f1231, asb(x16_1)); SB();
  s4 h1234 = TR(pW1, 45568);
  SB();
  u3006_2 = __builtin_amdgcn_exp2f(u3006_2); OPQ(u3006_2);
  SB();
  b8 f1232 = cat(l1232, h1232);
  MA(cL310, f1232, asb(x16_0)); SB();
  v3006_2 = __builtin_amdgcn_exp2f(v3006_2); OPQ(v3006_2);
  SB();
  MA(cL311, f1232, asb(x16_1)); SB();
  u3006_2 += 1.f; OPQ(u3006_2); SB(); v3006_2 += 1.f; OPQ(v3006_2);
  SB();
  u3006_2 = __builtin_amdgcn_rcpf(u3006_2); OPQ(u3006_2);
  SB();
  b8 f1233 = cat(l1233, h1233);
  MF(cL300, f1233, asb(x17_0)); SB();
  { unsigned t_; asm volatile("s_add_u32 m0, %[wb], 147456\n\ts_add_u32 %[t], %[sX2], 73728\n\tbuffer_load_dwordx4 %[vW2], %[rW2X], %[t] offen lds" : [t]"=&s"(t_) : [wb]"s"(wb), [sX2]"s"(sX2), [vW2]"v"(vW2), [rW2X]"s"(rW2X) : "memory", "scc"); }
  SB();
  MF(cL301, f1233, asb(x17_1)); SB();
  v3006_2 = __builtin_amdgcn_rcpf(v3006_2); OPQ(v3006_2);
  SB();
  b8 f1234 = cat(l1234, h1234);
  MF(cL310, f1234, asb(x17_0)); SB();
  u3006_2 *= cL211[0]; OPQ(u3006_2);
  SB();
  unsigned z3006_2 = pk(u3006_2, v3006_2 * cL211[1]); OPQ(z3006_2);
  SB();
  MF(cL311, f1234, asb(x17_1)); SB();
  float u3006_3 = cL211[2] * -1.4426950408889634f; OPQ(u3006_3); SB(); float v3006_3 = cL211[3] * -1.4426950408889634f; OPQ(v3006_3);
  SB();
  { unsigned kn_; const unsigned sR = Rb;
  { asm volatile("s_cmp_gt_u32 %[sR], 1\n\ts_cbranch_scc1 LN_%=\n\ts_waitcnt vmcnt(5)\n\ts_mov_b64 exec, 0xffff\n\ts_nop 7\n\tv_mov_b32 %[kn_], 0xff800000\n\tds_write_b32 %[pK_], %[kn_] offset:12\n\tds_write_b32 %[pK_], %[kn_] offset:1036\n\ts_cmp_gt_u32 %[sR], 0\n\ts_cbranch_scc1 LM_%=\n\tds_write_b32 %[pK_], %[kn_] offset:16396\n\tds_write_b32 %[pK_], %[kn_] offset:17420\n\tLM_%=:\n\ts_mov_b64 exec, -1\n\ts_waitcnt lgkmcnt(0)\n\tLN_%=:" : [kn_]"=&v"(kn_) : [sR]"s"(sR), [pK_]"v"(pK_) : "memory", "scc"); }
  }
  SB();
  { asm volatile("s_waitcnt vmcnt(5) lgkmcnt(0)\n\ts_barrier" :  :  : "memory"); }
  SB();
  f4 s000, s001, s010, s011, s020, s021, s030, s031, s040, s041, s050, s051, s060, s061, s070, s071, s080, s101, s110, s111, s120, s121, s130, s131, s140, s141, s150, s151, s160, s161, s170, s171, s180, s181, u000, u001, u010, u011, u020, u021, u030, u031, u040, u041, u050, u051, u060, u061, u070, u071, u080, u101, u110, u111, u120, u121, u130, u131, u140, u141, u150, u151, u160, u161, u170, u171, u180, u181, om0, om1, ol0, ol1, cd8, cn0, cn1, ce00, ce01, ce80, ce81;
  f4 dX00, dX01, dX10, dX11, dX20, dX21, dX30, dX31, dA00, dA01, dA10, dA11, dT0, dT1, dB00, dB01, dB10, dB11;
  float m00, m01, m10, m11;
  float NG = NEG; asm volatile("" : "+s"(NG));
  const unsigned sPV = 28311552u + ((Rb * 4 + j) * 4 + (gi ^ 1)) * 16384;
  const u4 k1487 = LR(pK_, 0);
  const u4 k1488 = LR(pK_, 1024);
  const u4 k1489 = LR(pK_, 4096);
  const u4 k1490 = LR(pK_, 5120);
  SB();
  if (cg) __builtin_amdgcn_s_setprio(1); else __builtin_amdgcn_s_setprio(0);
  SB();
  MI(s000, asb(k1487), qf0, cd0); SB();
  const u4 k1491 = LR(pK_, 8192);
  SB();
  { unsigned t_; asm volatile("s_add_u32 m0, %[wb], 155648\n\ts_add_u32 %[t], %[sX3], 73728\n\tbuffer_load_dwordx4 %[vW2], %[rW2X], %[t] offen lds" : [t]"=&s"(t_) : [wb]"s"(wb), [sX3]"s"(sX3), [vW2]"v"(vW2), [rW2X]"s"(rW2X) : "memory", "scc"); }
  SB();
  MZ(s001, asb(k1488), qf0); SB();
  const u4 k1492 = LR(pK_, 9216);
  SB();
  u3006_3 = __builtin_amdgcn_exp2f(u3006_3); OPQ(u3006_3);
  SB();
  v3006_3 = __builtin_amdgcn_exp2f(v3006_3); OPQ(v3006_3);
  SB();
  u3006_3 += 1.f; OPQ(u3006_3); SB(); v3006_3 += 1.f; OPQ(v3006_3);
  SB();
  u3006_3 = __builtin_amdgcn_rcpf(u3006_3); OPQ(u3006_3);
  SB();
  v3006_3 = __builtin_amdgcn_rcpf(v3006_3); OPQ(v3006_3);
  SB();
  MI(s101, asb(k1488), qf1, cd0); SB();
  MZ(s010, asb(k1489), qf0); SB();
  const u4 k1493 = LR(pK_, 12288);
  SB();
  { unsigned t_; asm volatile("s_add_u32 m0, %[wb], 131072\n\ts_mov_b32 %[t], 294912\n\tbuffer_load_dwordx4 %[vW2], %[rW2X], %[t] offen lds" : [t]"=&s"(t_) : [wb]"s"(wb), [vW2]"v"(vW2), [rW2X]"s"(rW2X) : "memory", "scc"); }
  SB();
  MZ(s110, asb(k1489), qf1); SB();
  m00 = mx3(s000[0], s000[1], NG); OPQ(m00);
  SB();
  m01 = mx3(s000[2], s000[3], NG); OPQ(m01);
  SB();
  u3006_3 *= cL211[2]; OPQ(u3006_3);
  SB();
  unsigned z3006_3 = pk(u3006_3, v3006_3 * cL211[3]); OPQ(z3006_3);
  SB();
  u4 hk21 = (u4){z3006_0, z3006_1, z3006_2, z3006_3}; OPQ(hk21);
  st16(hk21, rWS, v16, sH + (mF + 2) * 16384 + cg * 8192 + 1024);
  SB();
  float u3008_0 = cL300[0] * -1.4426950408889634f; OPQ(u3008_0); SB(); float v3008_0 = cL300[1] * -1.4426950408889634f; OPQ(v3008_0);
  SB();
  u3008_0 = __builtin_amdgcn_exp2f(u3008_0); OPQ(u3008_0);
  SB();
  MZ(s011, asb(k1490), qf0); SB();
  const u4 k1494 = LR(pK_, 13312);
  SB();
  m00 = mx3(m00, s001[0], s001[1]); OPQ(m00);
  m10 = mx3(s101[0], s101[1], NG); OPQ(m10);
  SB();
  m01 = mx3(m01, s001[2], s001[3]); OPQ(m01);
  m11 = mx3(s101[2], s101[3], NG); OPQ(m11);
  SB();
  v3008_0 = __builtin_amdgcn_exp2f(v3008_0); OPQ(v3008_0);
  SB();
  u3008_0 += 1.f; OPQ(u3008_0); SB(); v3008_0 += 1.f; OPQ(v3008_0);
  SB();
  u3008_0 = __builtin_amdgcn_rcpf(u3008_0); OPQ(u3008_0);
  SB();
  v3008_0 = __builtin_amdgcn_rcpf(v3008_0); OPQ(v3008_0);
  SB();
  u3008_0 *= cL300[0]; OPQ(u3008_0);
  SB();
  MZ(s111, asb(k1490), qf1); SB();
  { unsigned t_; asm volatile("s_add_u32 m0, %[wb], 139264\n\ts_mov_b32 %[t], 958464\n\tbuffer_load_dwordx4 %[vW2], %[rW2X], %[t] offen lds" : [t]"=&s"(t_) : [wb]"s"(wb), [vW2]"v"(vW2), [rW2X]"s"(rW2X) : "memory", "scc"); }
  SB();
  MZ(s020, asb(k1491), qf0); SB();
  const u4 k1495 = LR(pK_, 16384);
  SB();
  m00 = mx3(m00, s010[0], s010[1]); OPQ(m00);
  m10 = mx3(m10, s110[0], s110[1]); OPQ(m10);
  SB();
  m01 = mx3(m01, s010[2], s010[3]); OPQ(m01);
  m11 = mx3(m11, s110[2], s110[3]); OPQ(m11);
  SB();
  unsigned z3008_0 = pk(u3008_0, v3008_0 * cL300[1]); OPQ(z3008_0);
  SB();
  float u3008_1 = cL300[2] * -1.4426950408889634f; OPQ(u3008_1); SB(); float v3008_1 = cL300[3] * -1.4426950408889634f; OPQ(v3008_1);
  SB();
  u3008_1 = __builtin_amdgcn_exp2f(u3008_1); OPQ(u3008_1);
  SB();
  v3008_1 = __builtin_amdgcn_exp2f(v3008_1); OPQ(v3008_1);
  SB();
  u3008_1 += 1.f; OPQ(u3008_1); SB(); v3008_1 += 1.f; OPQ(v3008_1);
  SB();
  MZ(s120, asb(k1491), qf1); SB();
  MZ(s021, asb(k1492), qf0); SB();
  const u4 k1496 = LR(pK_, 17408);
  SB();
  { unsigned t_; asm volatile("s_add_u32 m0, %[wb], 122880\n\ts_add_u32 %[t], %[sX0], 147456\n\tbuffer_load_dwordx4 %[vW2], %[rW2X], %[t] offen lds" : [t]"=&s"(t_) : [wb]"s"(wb), [sX0]"s"(sX0), [vW2]"v"(vW2), [rW2X]"s"(rW2X) : "memory", "scc"); }
  SB();
  MZ(s121, asb(k1492), qf1); SB();
  m00 = mx3(m00, s011[0], s011[1]); OPQ(m00);
  m10 = mx3(m10, s111[0], s111[1]); OPQ(m10);
  SB();
  m01 = mx3(m01, s011[2], s011[3]); OPQ(m01);
  m11 = mx3(m11, s111[2], s111[3]); OPQ(m11);
  SB();
  u3008_1 = __builtin_amdgcn_rcpf(u3008_1); OPQ(u3008_1);
  SB();
  v3008_1 = __builtin_amdgcn_rcpf(v3008_1); OPQ(v3008_1);
  SB();
  u3008_1 *= cL300[2]; OPQ(u3008_1);
  SB();
  unsigned z3008_1 = pk(u3008_1, v3008_1 * cL300[3]); OPQ(z3008_1);
  SB();
  float u3008_2 = cL310[0] * -1.4426950408889634f; OPQ(u3008_2); SB(); float v3008_2 = cL310[1] * -1.4426950408889634f; OPQ(v3008_2);
  SB();
  MZ(s030, asb(k1493), qf0); SB();
  const u4 k1497 = LR(pK_, 20480);
  SB();
  m00 = mx3(m00, s020[0], s020[1]); OPQ(m00);
  m10 = mx3(m10, s120[0], s120[1]); OPQ(m10);
  SB();
  m01 = mx3(m01, s020[2], s020[3]); OPQ(m01);
  m11 = mx3(m11, s120[2], s120[3]); OPQ(m11);
  SB();
  u3008_2 = __builtin_amdgcn_exp2f(u3008_2); OPQ(u3008_2);
  SB();
  v3008_2 = __builtin_amdgcn_exp2f(v3008_2); OPQ(v3008_2);
  SB();
  u3008_2 += 1.f; OPQ(u3008_2); SB(); v3008_2 += 1.f; OPQ(v3008_2);
  SB();
  u3008_2 = __builtin_amdgcn_rcpf(u3008_2); OPQ(u3008_2);
  SB();
  v3008_2 = __builtin_amdgcn_rcpf(v3008_2); OPQ(v3008_2);
  SB();
  MZ(s130, asb(k1493), qf1); SB();
  MZ(s031, asb(k1494), qf0); SB();
  const u4 k1498 = LR(pK_, 21504);
  SB();
  m00 = mx3(m00, s021[0], s021[1]); OPQ(m00);
  m10 = mx3(m10, s121[0], s121[1]); OPQ(m10);
  SB();
  m01 = mx3(m01, s021[2], s021[3]); OPQ(m01);
  m11 = mx3(m11, s121[2], s121[3]); OPQ(m11);
  SB();
  u3008_2 *= cL310[0]; OPQ(u3008_2);
  SB();
  unsigned z3008_2 = pk(u3008_2, v3008_2 * cL310[1]); OPQ(z3008_2);
  SB();
  float u3008_3 = cL310[2] * -1.4426950408889634f; OPQ(u3008_3); SB(); float v3008_3 = cL310[3] * -1.4426950408889634f; OPQ(v3008_3);
  SB();
  u3008_3 = __builtin_amdgcn_exp2f(u3008_3); OPQ(u3008_3);
  SB();
  v3008_3 = __builtin_amdgcn_exp2f(v3008_3); OPQ(v3008_3);
  SB();
  MZ(s131, asb(k1494), qf1); SB();
  MZ(s040, asb(k1495), qf0); SB();
  const u4 k1499 = LR(pK_, 24576);
  SB();
  m00 = mx3(m00, s030[0], s030[1]); OPQ(m00);
  m10 = mx3(m10, s130[0], s130[1]); OPQ(m10);
  SB();
  m01 = mx3(m01, s030[2], s030[3]); OPQ(m01);
  m11 = mx3(m11, s130[2], s130[3]); OPQ(m11);
  SB();
  u3008_3 += 1.f; OPQ(u3008_3); SB(); v3008_3 += 1.f; OPQ(v3008_3);
  SB();
  u3008_3 = __builtin_amdgcn_rcpf(u3008_3); OPQ(u3008_3);
  SB();
  v3008_3 = __builtin_amdgcn_rcpf(v3008_3); OPQ(v3008_3);
  SB();
  u3008_3 *= cL310[2]; OPQ(u3008_3);
  SB();
  unsigned z3008_3 = pk(u3008_3, v3008_3 * cL310[3]); OPQ(z3008_3);
  SB();
  MZ(s140, asb(k1495), qf1); SB();
  MZ(s041, asb(k1496), qf0); SB();
  const u4 k1500 = LR(pK_, 25600);
  SB();
  m00 = mx3(m00, s031[0], s031[1]); OPQ(m00);
  m10 = mx3(m10, s131[0], s131[1]); OPQ(m10);
  SB();
  m01 = mx3(m01, s031[2], s031[3]); OPQ(m01);
  m11 = mx3(m11, s131[2], s131[3]); OPQ(m11);
  SB();
  u4 hk30 = (u4){z3008_0, z3008_1, z3008_2, z3008_3}; OPQ(hk30);
  st16(hk30, rWS, v16, sH + (mF + 3) * 16384 + cg * 8192 + 0);
  SB();
  float u3009_0 = cL301[0] * -1.4426950408889634f; OPQ(u3009_0); SB(); float v3009_0 = cL301[1] * -1.4426950408889634f; OPQ(v3009_0);
  SB();
  u3009_0 = __builtin_amdgcn_exp2f(u3009_0); OPQ(u3009_0);
  SB();
  v3009_0 = __builtin_amdgcn_exp2f(v3009_0); OPQ(v3009_0);
  SB();
  u3009_0 += 1.f; OPQ(u3009_0); SB(); v3009_0 += 1.f; OPQ(v3009_0);
  SB();
  MZ(s141, asb(k1496), qf1); SB();
  MZ(s050, asb(k1497), qf0); SB();
  const u4 k1501 = LR(pK_, 28672);
  SB();
  unsigned v7 = v16 & 0x70; OPQ(v7);
  u2 pHA;
  { asm volatile("buffer_load_dwordx2 %[pHA], %[v7], %[rWS], %[sFH] offen offset:0 sc1" : [pHA]"=&v"(pHA) : [v7]"v"(v7), [rWS]"s"(rWS), [sFH]"s"(sFH) : "memory"); }
  SB();
  MZ(s150, asb(k1497), qf1); SB();
  m00 = mx3(m00, s040[0], s040[1]); OPQ(m00);
  m10 = mx3(m10, s140[0], s140[1]); OPQ(m10);
  SB();
  m01 = mx3(m01, s040[2], s040[3]); OPQ(m01);
  m11 = mx3(m11, s140[2], s140[3]); OPQ(m11);
  SB();
  u3009_0 = __builtin_amdgcn_rcpf(u3009_0); OPQ(u3009_0);
  SB();
  v3009_0 = __builtin_amdgcn_rcpf(v3009_0); OPQ(v3009_0);
  SB();
  u3009_0 *= cL301[0]; OPQ(u3009_0);
  SB();
  unsigned z3009_0 = pk(u3009_0, v3009_0 * cL301[1]); OPQ(z3009_0);
  SB();
  float u3009_1 = cL301[2] * -1.4426950408889634f; OPQ(u3009_1); SB(); float v3009_1 = cL301[3] * -1.4426950408889634f; OPQ(v3009_1);
  SB();
  MZ(s051, asb(k1498), qf0); SB();
  const u4 k1502 = LR(pK_, 29696);
  SB();
  m00 = mx3(m00, s041[0], s041[1]); OPQ(m00);
  m10 = mx3(m10, s141[0], s141[1]); OPQ(m10);
  SB();
  m01 = mx3(m01, s041[2], s041[3]); OPQ(m01);
  m11 = mx3(m11, s141[2], s141[3]); OPQ(m11);
  SB();
  cd8 = __builtin_bit_cast(f4, __builtin_bit_cast(u4, cd0) ^ 0xff800000u); OPQ(cd8);
  SB();
  u3009_1 = __builtin_amdgcn_exp2f(u3009_1); OPQ(u3009_1);
  SB();
  v3009_1 = __builtin_amdgcn_exp2f(v3009_1); OPQ(v3009_1);
  SB();
  u3009_1 += 1.f; OPQ(u3009_1); SB(); v3009_1 += 1.f; OPQ(v3009_1);
  SB();
  u3009_1 = __builtin_amdgcn_rcpf(u3009_1); OPQ(u3009_1);
  SB();
  MZ(s151, asb(k1498), qf1); SB();
  v3009_1 = __builtin_amdgcn_rcpf(v3009_1); OPQ(v3009_1);
  SB();
  MZ(s060, asb(k1499), qf0); SB();
  const u4 k1503 = LR(pK_, 32768);
  SB();
  m00 = mx3(m00, s050[0], s050[1]); OPQ(m00);
  m10 = mx3(m10, s150[0], s150[1]); OPQ(m10);
  SB();
  m01 = mx3(m01, s050[2], s050[3]); OPQ(m01);
  m11 = mx3(m11, s150[2], s150[3]); OPQ(m11);
  SB();
  u3009_1 *= cL301[2]; OPQ(u3009_1);
  SB();
  unsigned z3009_1 = pk(u3009_1, v3009_1 * cL301[3]); OPQ(z3009_1);
  SB();
  float u3009_2 = cL311[0] * -1.4426950408889634f; OPQ(u3009_2); SB(); float v3009_2 = cL311[1] * -1.4426950408889634f; OPQ(v3009_2);
  SB();
  u3009_2 = __builtin_amdgcn_exp2f(u3009_2); OPQ(u3009_2);
  SB();
  v3009_2 = __builtin_amdgcn_exp2f(v3009_2); OPQ(v3009_2);
  SB();
  MZ(s160, asb(k1499), qf1); SB();
  MZ(s061, asb(k1500), qf0); SB();
  const u4 k1504 = LR(pK_, 33792);
  SB();
  m00 = mx3(m00, s051[0], s051[1]); OPQ(m00);
  m10 = mx3(m10, s151[0], s151[1]); OPQ(m10);
  SB();
  m01 = mx3(m01, s051[2], s051[3]); OPQ(m01);
  m11 = mx3(m11, s151[2], s151[3]); OPQ(m11);
  SB();
  unsigned mk1 = LN(0xffffffffull) ? 0u : 0xffff0000u; OPQ(mk1); SB(); unsigned mk2 = LN(0xffffffffull) ? ~0u : mk1; OPQ(mk2);
  SB();
  u3009_2 += 1.f; OPQ(u3009_2); SB(); v3009_2 += 1.f; OPQ(v3009_2);
  SB();
  u3009_2 = __builtin_amdgcn_rcpf(u3009_2); OPQ(u3009_2);
  SB();
  v3009_2 = __builtin_amdgcn_rcpf(v3009_2); OPQ(v3009_2);
  SB();
  u3009_2 *= cL311[0]; OPQ(u3009_2);
  SB();
  unsigned z3009_2 = pk(u3009_2, v3009_2 * cL311[1]); OPQ(z3009_2);
  SB();
  MZ(s161, asb(k1500), qf1); SB();
  MZ(s070, asb(k1501), qf0); SB();
  const u4 k1505 = LR(pK_, 0);
  SB();
  m00 = mx3(m00, s060[0], s060[1]); OPQ(m00);
  m10 = mx3(m10, s160[0], s160[1]); OPQ(m10);
  SB();
  m01 = mx3(m01, s060[2], s060[3]); OPQ(m01);
  m11 = mx3(m11, s160[2], s160[3]); OPQ(m11);
  SB();
  float u3009_3 = cL311[2] * -1.4426950408889634f; OPQ(u3009_3); SB(); float v3009_3 = cL311[3] * -1.4426950408889634f; OPQ(v3009_3);
  SB();
  u3009_3 = __builtin_amdgcn_exp2f(u3009_3); OPQ(u3009_3);
  SB();
  v3009_3 = __builtin_amdgcn_exp2f(v3009_3); OPQ(v3009_3);
  SB();
  u3009_3 += 1.f; OPQ(u3009_3); SB(); v3009_3 += 1.f; OPQ(v3009_3);
  SB();
  u3009_3 = __builtin_amdgcn_rcpf(u3009_3); OPQ(u3009_3);
  SB();
  MZ(s170, asb(k1501), qf1); SB();
  MZ(s071, asb(k1502), qf0); SB();
  const u4 k1506 = LR(pK_, 1024);
  SB();
  m00 = mx3(m00, s061[0], s061[1]); OPQ(m00);
  m10 = mx3(m10, s161[0], s161[1]); OPQ(m10);
  SB();
  m01 = mx3(m01, s061[2], s061[3]); OPQ(m01);
  m11 = mx3(m11, s161[2], s161[3]); OPQ(m11);
  SB();
  unsigned pVL = ((v16 >> 2) & 0xf0) | (lb + (r * 2 + cg) * 2048 + 264); OPQ(pVL);
  SB();
  unsigned pV0 = lb + 49152 + (r * 2 + cg) * 1024 + v16 + ((v16 >> 5) & 8), pV1 = pV0 ^ 8; OPQ(pV0); OPQ(pV1);
  SB();
  v3009_3 = __builtin_amdgcn_rcpf(v3009_3); OPQ(v3009_3);
  SB();
  u3009_3 *= cL311[2]; OPQ(u3009_3);
  SB();
  unsigned z3009_3 = pk(u3009_3, v3009_3 * cL311[3]); OPQ(z3009_3);
  SB();
  u4 hk31 = (u4){z3009_0, z3009_1, z3009_2, z3009_3}; OPQ(hk31);
  st16(hk31, rWS, v16, sH + (mF + 3) * 16384 + cg * 8192 + 1024);
  SB();
  MZ(s171, asb(k1502), qf1); SB();
  MI(s080, asb(k1503), qf0, cd8); SB();
  const u4 k1507 = LR(pK_, 4096);
  SB();
  { asm volatile("s_waitcnt vmcnt(5)" :  :  : "memory"); }
  st8((u2){tlo, thi}, rWS, vF + 128, sGH); SB();
  SB();
  MZ(s180, asb(k1503), qf1); SB();
  { asm volatile("s_waitcnt vmcnt(2)" : [pHA]"+v"(pHA) :  : "memory"); }
  repoll<0>(pHA, rWS, v7, sFH, T_); SB();
  SB();
  MI(s181, asb(k1504), qf1, cd8); SB();
  const u4 k1508 = LR(pK_, 5120);
  SB();
  u4 h1_0, h1_1;
  { unsigned t_; asm volatile("s_xor_b32 %[t], %[sJ], 65536\n\ts_nop 2\n\tbuffer_load_dwordx4 %[h1_0], %[v16], %[rH], %[t] offen sc1\n\tbuffer_load_dwordx4 %[h1_1], %[v16], %[rH], %[t] offen offset:1024 sc1" : [t]"=&s"(t_), [h1_0]"=&v"(h1_0), [h1_1]"=&v"(h1_1) : [sJ]"s"(sJ), [v16]"v"(v16), [rH]"s"(rH) : "memory", "scc"); }
  SB();
  SB();
  m00 = mx3(m00, s070[0], s070[1]); OPQ(m00);
  m10 = mx3(m10, s170[0], s170[1]); OPQ(m10);
  SB();
  m01 = mx3(m01, s070[2], s070[3]); OPQ(m01);
  m11 = mx3(m11, s170[2], s170[3]); OPQ(m11);
  SB();
  m00 = mx3(m00, s071[0], s071[1]); OPQ(m00);
  m10 = mx3(m10, s171[0], s171[1]); OPQ(m10);
  SB();
  m01 = mx3(m01, s071[2], s071[3]); OPQ(m01);
  m11 = mx3(m11, s171[2], s171[3]); OPQ(m11);
  SB();
  m00 = mx3(m00, s080[0], s080[1]); OPQ(m00);
  m10 = mx3(m10, s180[0], s180[1]); OPQ(m10);
  SB();
  m01 = mx3(m01, s080[2], s080[3]); OPQ(m01);
  m11 = mx3(m11, s180[2], s180[3]); OPQ(m11);
  SB();
  m10 = mx3(m10, s181[0], s181[1]); OPQ(m10);
  SB();
  m11 = mx3(m11, s181[2], s181[3]); OPQ(m11);
  SB();
  SB();
  m00 = mx3(m00, m01, NG); OPQ(m00); SB(); m10 = mx3(m10, m11, NG); OPQ(m10);
  SB();
  SB();
  { auto r_ = __builtin_amdgcn_permlane16_swap(f2u(m00), f2u(m10), false, false); m00 = mx3(u2f(r_[0]), u2f(r_[1]), NG); }
  { auto r_ = __builtin_amdgcn_permlane32_swap(f2u(m00), f2u(m00), false, false); m00 = __builtin_fminf(__builtin_fminf(-u2f(r_[0]), -u2f(r_[1])), -NG); }
  m00 -= u2f(f2u(m00) & mk1); m10 = m00 - u2f(f2u(m00) & mk2);
  { auto r_ = __builtin_amdgcn_permlane16_swap(f2u(m00), f2u(m10), false, false); one0 = __builtin_amdgcn_alignbit(0x3f80u, r_[0], 16); one1 = __builtin_amdgcn_alignbit(0x3f80u, r_[1], 16); }
  const b8 qg0 = asb((u4){qm00, qm02, ql0, one0}), qg1 = asb((u4){qm10, qm12, ql1, one1});
  SB();
  SB();
  MI(u000, asb(k1505), qg0, cd0); SB();
  s4 l1510 = TR(pVL, 0);
  s4 h1510 = TR(pVL, 1024);
  SB();
  MZ(u001, asb(k1506), qg0); SB();
  s4 l1509 = TR(pV0, 0);
  SB();
  MI(u101, asb(k1506), qg1, cd0); SB();
  s4 h1509 = TR(pV1, 0);
  SB();
  MZ(u010, asb(k1507), qg0); SB();
  const u4 k1511 = LR(pK_, 8192);
  SB();
  u4 h2_0, h2_1;
  { unsigned t_; asm volatile("s_xor_b32 %[t], %[sJ], 131072\n\ts_nop 2\n\tbuffer_load_dwordx4 %[h2_0], %[v16], %[rH], %[t] offen sc1\n\tbuffer_load_dwordx4 %[h2_1], %[v16], %[rH], %[t] offen offset:1024 sc1" : [t]"=&s"(t_), [h2_0]"=&v"(h2_0), [h2_1]"=&v"(h2_1) : [sJ]"s"(sJ), [v16]"v"(v16), [rH]"s"(rH) : "memory", "scc"); }
  SB();
  SB();
  MZ(u110, asb(k1507), qg1); SB();
  float e0000 = __builtin_amdgcn_exp2f(u000[0]); OPQ(e0000);
  SB();
  float e0001 = __builtin_amdgcn_exp2f(u000[1]); OPQ(e0001);
  SB();
  float e0002 = __builtin_amdgcn_exp2f(u000[2]); OPQ(e0002);
  SB();
  float e0003 = __builtin_amdgcn_exp2f(u000[3]); OPQ(e0003);
  SB();
  unsigned y0000 = pk(e0000, e0001); OPQ(y0000);
  SB();
  unsigned y0001 = pk(e0002, e0003); OPQ(y0001);
  SB();
  const unsigned y1000 = zz(), y1001 = zz();
  SB();
  MZ(u011, asb(k1508), qg0); SB();
  const u4 k1512 = LR(pK_, 9216);
  SB();
  float e0010 = __builtin_amdgcn_exp2f(u001[0]); OPQ(e0010);
  SB();
  float e0011 = __builtin_amdgcn_exp2f(u001[1]); OPQ(e0011);
  SB();
  float e0012 = __builtin_amdgcn_exp2f(u001[2]); OPQ(e0012);
  SB();
  float e0013 = __builtin_amdgcn_exp2f(u001[3]); OPQ(e0013);
  SB();
  unsigned y0010 = pk(e0010, e0011); OPQ(y0010);
  SB();
  unsigned y0011 = pk(e0012, e0013); OPQ(y0011);
  SB();
  float e1010 = __builtin_amdgcn_exp2f(u101[0]); OPQ(e1010);
  SB();
  float e1011 = __builtin_amdgcn_exp2f(u101[1]); OPQ(e1011);
  SB();
  float e1012 = __builtin_amdgcn_exp2f(u101[2]); OPQ(e1012);
  SB();
  float e1013 = __builtin_amdgcn_exp2f(u101[3]); OPQ(e1013);
  SB();
  unsigned y1010 = pk(e1010, e1011); OPQ(y1010);
  SB();
  unsigned y1011 = pk(e1012, e1013); OPQ(y1011);
  SB();
  MZ(u111, asb(k1508), qg1); SB();
  const u4 p00 = {y0000, y0001, y0010, y0011};
  const u4 p10 = {y1000, y1001, y1010, y1011};
  SB();
  b8 f1510 = cat(l1510, h1510);
  MZ(ol0, f1510, asb(p00)); SB();
  s4 l1514 = TR(pVL, 4096);
  SB();
  float e0100 = __builtin_amdgcn_exp2f(u010[0]); OPQ(e0100);
  SB();
  float e0101 = __builtin_amdgcn_exp2f(u010[1]); OPQ(e0101);
  SB();
  float e0102 = __builtin_amdgcn_exp2f(u010[2]); OPQ(e0102);
  SB();
  float e0103 = __builtin_amdgcn_exp2f(u010[3]); OPQ(e0103);
  SB();
  unsigned y0100 = pk(e0100, e0101); OPQ(y0100);
  SB();
  unsigned y0101 = pk(e0102, e0103); OPQ(y0101);
  SB();
  float e1100 = __builtin_amdgcn_exp2f(u110[0]); OPQ(e1100);
  SB();
  MZ(ol1, f1510, asb(p10)); SB();
  s4 h1514 = TR(pVL, 5120);
  SB();
  float e1101 = __builtin_amdgcn_exp2f(u110[1]); OPQ(e1101);
  SB();
  float e1102 = __builtin_amdgcn_exp2f(u110[2]); OPQ(e1102);
  SB();
  b8 f1509 = cat(l1509, h1509);
  MZ(om0, f1509, asb(p00)); SB();
  s4 l1513 = TR(pV0, 2048);
  SB();
  float e1103 = __builtin_amdgcn_exp2f(u110[3]); OPQ(e1103);
  SB();
  unsigned y1100 = pk(e1100, e1101); OPQ(y1100);
  SB();
  unsigned y1101 = pk(e1102, e1103); OPQ(y1101);
  SB();
  MZ(om1, f1509, asb(p10)); SB();
  s4 h1513 = TR(pV1, 2048);
  SB();
  float e0110 = __builtin_amdgcn_exp2f(u011[0]); OPQ(e0110);
  SB();
  float e0111 = __builtin_amdgcn_exp2f(u011[1]); OPQ(e0111);
  SB();
  float e0112 = __builtin_amdgcn_exp2f(u011[2]); OPQ(e0112);
  SB();
  MZ(u020, asb(k1511), qg0); SB();
  const u4 k1515 = LR(pK_, 12288);
  SB();
  u4 h3_0, h3_1;
  { unsigned t_; asm volatile("s_xor_b32 %[t], %[sJ], 196608\n\ts_nop 2\n\tbuffer_load_dwordx4 %[h3_0], %[v16], %[rH], %[t] offen sc1\n\tbuffer_load_dwordx4 %[h3_1], %[v16], %[rH], %[t] offen offset:1024 sc1" : [t]"=&s"(t_), [h3_0]"=&v"(h3_0), [h3_1]"=&v"(h3_1) : [sJ]"s"(sJ), [v16]"v"(v16), [rH]"s"(rH) : "memory", "scc"); }
  SB();
  SB();
  MZ(u120, asb(k1511), qg1); SB();
  float e0113 = __builtin_amdgcn_exp2f(u011[3]); OPQ(e0113);
  SB();
  unsigned y0110 = pk(e0110, e0111); OPQ(y0110);
  SB();
  unsigned y0111 = pk(e0112, e0113); OPQ(y0111);
  SB();
  float e1110 = __builtin_amdgcn_exp2f(u111[0]); OPQ(e1110);
  SB();
  float e1111 = __builtin_amdgcn_exp2f(u111[1]); OPQ(e1111);
  SB();
  MZ(u021, asb(k1512), qg0); SB();
  const u4 k1516 = LR(pK_, 13312);
  SB();
  float e1112 = __builtin_amdgcn_exp2f(u111[2]); OPQ(e1112);
  SB();
  float e1113 = __builtin_amdgcn_exp2f(u111[3]); OPQ(e1113);
  SB();
  MZ(u121, asb(k1512), qg1); SB();
  unsigned y1110 = pk(e1110, e1111); OPQ(y1110);
  SB();
  unsigned y1111 = pk(e1112, e1113); OPQ(y1111);
  SB();
  const u4 p01 = {y0100, y0101, y0110, y0111};
  const u4 p11 = {y1100, y1101, y1110, y1111};
  SB();
  b8 f1514 = cat(l1514, h1514);
  MF(ol0, f1514, asb(p01)); SB();
  s4 l1518 = TR(pVL, 8192);
  SB();
  float e0200 = __builtin_amdgcn_exp2f(u020[0]); OPQ(e0200);
  SB();
  float e0201 = __builtin_amdgcn_exp2f(u020[1]); OPQ(e0201);
  SB();
  float e0202 = __builtin_amdgcn_exp2f(u020[2]); OPQ(e0202);
  SB();
  MF(ol1, f1514, asb(p11)); SB();
  s4 h1518 = TR(pVL, 9216);
  SB();
  float e0203 = __builtin_amdgcn_exp2f(u020[3]); OPQ(e0203);
  SB();
  unsigned y0200 = pk(e0200, e0201); OPQ(y0200);
  SB();
  unsigned y0201 = pk(e0202, e0203); OPQ(y0201);
  SB();
  b8 f1513 = cat(l1513, h1513);
  MF(om0, f1513, asb(p01)); SB();
  s4 l1517 = TR(pV0, 4096);
  SB();
  float e1200 = __builtin_amdgcn_exp2f(u120[0]); OPQ(e1200);
  SB();
  float e1201 = __builtin_amdgcn_exp2f(u120[1]); OPQ(e1201);
  SB();
  float e1202 = __builtin_amdgcn_exp2f(u120[2]); OPQ(e1202);
  SB();
  MF(om1, f1513, asb(p11)); SB();
  s4 h1517 = TR(pV1, 4096);
  SB();
  float e1203 = __builtin_amdgcn_exp2f(u120[3]); OPQ(e1203);
  SB();
  unsigned y1200 = pk(e1200, e1201); OPQ(y1200);
  SB();
  unsigned y1201 = pk(e1202, e1203); OPQ(y1201);
  SB();
  float e0210 = __builtin_amdgcn_exp2f(u021[0]); OPQ(e0210);
  SB();
  MZ(u030, asb(k1515), qg0); SB();
  const u4 k1519 = LR(pK_, 16384);
  SB();
  u4 h5_0, h5_1;
  { unsigned t_; asm volatile("s_xor_b32 %[t], %[sJ], 81920\n\ts_nop 2\n\tbuffer_load_dwordx4 %[h5_0], %[v16], %[rH], %[t] offen sc1\n\tbuffer_load_dwordx4 %[h5_1], %[v16], %[rH], %[t] offen offset:1024 sc1" : [t]"=&s"(t_), [h5_0]"=&v"(h5_0), [h5_1]"=&v"(h5_1) : [sJ]"s"(sJ), [v16]"v"(v16), [rH]"s"(rH) : "memory", "scc"); }
  SB();
  SB();
  MZ(u130, asb(k1515), qg1); SB();
  { asm volatile("s_waitcnt vmcnt(9)" :  :  : "memory"); }
  st8((u2){tlo, thi}, rWS, vF + 256, sGH); SB();
  SB();
  MZ(u031, asb(k1516), qg0); SB();
  const u4 k1520 = LR(pK_, 17408);
  SB();
  float e0211 = __builtin_amdgcn_exp2f(u021[1]); OPQ(e0211);
  SB();
  float e0212 = __builtin_amdgcn_exp2f(u021[2]); OPQ(e0212);
  SB();
  float e0213 = __builtin_amdgcn_exp2f(u021[3]); OPQ(e0213);
  SB();
  unsigned y0210 = pk(e0210, e0211); OPQ(y0210);
  SB();
  unsigned y0211 = pk(e0212, e0213); OPQ(y0211);
  SB();
  float e1210 = __builtin_amdgcn_exp2f(u121[0]); OPQ(e1210);
  SB();
  float e1211 = __builtin_amdgcn_exp2f(u121[1]); OPQ(e1211);
  SB();
  float e1212 = __builtin_amdgcn_exp2f(u121[2]); OPQ(e1212);
  SB();
  float e1213 = __builtin_amdgcn_exp2f(u121[3]); OPQ(e1213);
  SB();
  unsigned y1210 = pk(e1210, e1211); OPQ(y1210);
  SB();
  unsigned y1211 = pk(e1212, e1213); OPQ(y1211);
  SB();
  MZ(u131, asb(k1516), qg1); SB();
  const u4 p02 = {y0200, y0201, y0210, y0211};
  const u4 p12 = {y1200, y1201, y1210, y1211};
  SB();
  b8 f1518 = cat(l1518, h1518);
  MF(ol0, f1518, asb(p02)); SB();
  s4 l1522 = TR(pVL, 12288);
  SB();
  float e0300 = __builtin_amdgcn_exp2f(u030[0]); OPQ(e0300);
  SB();
  float e0301 = __builtin_amdgcn_exp2f(u030[1]); OPQ(e0301);
  SB();
  float e0302 = __builtin_amdgcn_exp2f(u030[2]); OPQ(e0302);
  SB();
  float e0303 = __builtin_amdgcn_exp2f(u030[3]); OPQ(e0303);
  SB();
  unsigned y0300 = pk(e0300, e0301); OPQ(y0300);
  SB();
  unsigned y0301 = pk(e0302, e0303); OPQ(y0301);
  SB();
  float e1300 = __builtin_amdgcn_exp2f(u130[0]); OPQ(e1300);
  SB();
  MF(ol1, f1518, asb(p12)); SB();
  s4 h1522 = TR(pVL, 13312);
  SB();
  float e1301 = __builtin_amdgcn_exp2f(u130[1]); OPQ(e1301);
  SB();
  float e1302 = __builtin_amdgcn_exp2f(u130[2]); OPQ(e1302);
  SB();
  b8 f1517 = cat(l1517, h1517);
  MF(om0, f1517, asb(p02)); SB();
  s4 l1521 = TR(pV0, 6144);
  SB();
  float e1303 = __builtin_amdgcn_exp2f(u130[3]); OPQ(e1303);
  SB();
  unsigned y1300 = pk(e1300, e1301); OPQ(y1300);
  SB();
  unsigned y1301 = pk(e1302, e1303); OPQ(y1301);
  SB();
  MF(om1, f1517, asb(p12)); SB();
  s4 h1521 = TR(pV1, 6144);
  SB();
  float e0310 = __builtin_amdgcn_exp2f(u031[0]); OPQ(e0310);
  SB();
  float e0311 = __builtin_amdgcn_exp2f(u031[1]); OPQ(e0311);
  SB();
  float e0312 = __builtin_amdgcn_exp2f(u031[2]); OPQ(e0312);
  SB();
  MZ(u040, asb(k1519), qg0); SB();
  const u4 k1523 = LR(pK_, 20480);
  SB();
  u4 h6_0, h6_1;
  { unsigned t_; asm volatile("s_xor_b32 %[t], %[sJ], 147456\n\ts_nop 2\n\tbuffer_load_dwordx4 %[h6_0], %[v16], %[rH], %[t] offen sc1\n\tbuffer_load_dwordx4 %[h6_1], %[v16], %[rH], %[t] offen offset:1024 sc1" : [t]"=&s"(t_), [h6_0]"=&v"(h6_0), [h6_1]"=&v"(h6_1) : [sJ]"s"(sJ), [v16]"v"(v16), [rH]"s"(rH) : "memory", "scc"); }
  SB();
  SB();
  MZ(u140, asb(k1519), qg1); SB();
  float e0313 = __builtin_amdgcn_exp2f(u031[3]); OPQ(e0313);
  SB();
  unsigned y0310 = pk(e0310, e0311); OPQ(y0310);
  SB();
  unsigned y0311 = pk(e0312, e0313); OPQ(y0311);
  SB();
  float e1310 = __builtin_amdgcn_exp2f(u131[0]); OPQ(e1310);
  SB();
  float e1311 = __builtin_amdgcn_exp2f(u131[1]); OPQ(e1311);
  SB();
  MZ(u041, asb(k1520), qg0); SB();
  const u4 k1524 = LR(pK_, 21504);
  SB();
  float e1312 = __builtin_amdgcn_exp2f(u131[2]); OPQ(e1312);
  SB();
  float e1313 = __builtin_amdgcn_exp2f(u131[3]); OPQ(e1313);
  SB();
  MZ(u141, asb(k1520), qg1); SB();
  unsigned y1310 = pk(e1310, e1311); OPQ(y1310);
  SB();
  unsigned y1311 = pk(e1312, e1313); OPQ(y1311);
  SB();
  const u4 p03 = {y0300, y0301, y0310, y0311};
  const u4 p13 = {y1300, y1301, y1310, y1311};
  SB();
  __builtin_amdgcn_s_setprio(0);
  SB();
  b8 f1522 = cat(l1522, h1522);
  MF(ol0, f1522, asb(p03)); SB();
  s4 l1526 = TR(pVL, 16384);
  SB();
  float e0400 = __builtin_amdgcn_exp2f(u040[0]); OPQ(e0400);
  SB();
  float e0401 = __builtin_amdgcn_exp2f(u040[1]); OPQ(e0401);
  SB();
  float e0402 = __builtin_amdgcn_exp2f(u040[2]); OPQ(e0402);
  SB();
  MF(ol1, f1522, asb(p13)); SB();
  s4 h1526 = TR(pVL, 17408);
  SB();
  float e0403 = __builtin_amdgcn_exp2f(u040[3]); OPQ(e0403);
  SB();
  unsigned y0400 = pk(e0400, e0401); OPQ(y0400);
  SB();
  unsigned y0401 = pk(e0402, e0403); OPQ(y0401);
  SB();
  b8 f1521 = cat(l1521, h1521);
  MF(om0, f1521, asb(p03)); SB();
  s4 l1525 = TR(pV0, 8192);
  SB();
  float e1400 = __builtin_amdgcn_exp2f(u140[0]); OPQ(e1400);
  SB();
  float e1401 = __builtin_amdgcn_exp2f(u140[1]); OPQ(e1401);
  SB();
  float e1402 = __builtin_amdgcn_exp2f(u140[2]); OPQ(e1402);
  SB();
  MF(om1, f1521, asb(p13)); SB();
  s4 h1525 = TR(pV1, 8192);
  SB();
  float e1403 = __builtin_amdgcn_exp2f(u140[3]); OPQ(e1403);
  SB();
  unsigned y1400 = pk(e1400, e1401); OPQ(y1400);
  SB();
  unsigned y1401 = pk(e1402, e1403); OPQ(y1401);
  SB();
  float e0410 = __builtin_amdgcn_exp2f(u041[0]); OPQ(e0410);
  SB();
  SB();
  MZ(u050, asb(k1523), qg0); SB();
  const u4 k1527 = LR(pK_, 24576);
  SB();
  u4 h7_0, h7_1;
  { unsigned t_; asm volatile("s_xor_b32 %[t], %[sJ], 212992\n\ts_nop 2\n\tbuffer_load_dwordx4 %[h7_0], %[v16], %[rH], %[t] offen sc1\n\tbuffer_load_dwordx4 %[h7_1], %[v16], %[rH], %[t] offen offset:1024 sc1" : [t]"=&s"(t_), [h7_0]"=&v"(h7_0), [h7_1]"=&v"(h7_1) : [sJ]"s"(sJ), [v16]"v"(v16), [rH]"s"(rH) : "memory", "scc"); }
  SB();
  SB();
  MZ(u150, asb(k1523), qg1); SB();
  float e0411 = __builtin_amdgcn_exp2f(u041[1]); OPQ(e0411);
  SB();
  float e0412 = __builtin_amdgcn_exp2f(u041[2]); OPQ(e0412);
  SB();
  float e0413 = __builtin_amdgcn_exp2f(u041[3]); OPQ(e0413);
  SB();
  unsigned y0410 = pk(e0410, e0411); OPQ(y0410);
  SB();
  unsigned y0411 = pk(e0412, e0413); OPQ(y0411);
  SB();
  float e1410 = __builtin_amdgcn_exp2f(u141[0]); OPQ(e1410);
  SB();
  MZ(u051, asb(k1524), qg0); SB();
  const u4 k1528 = LR(pK_, 25600);
  SB();
  float e1411 = __builtin_amdgcn_exp2f(u141[1]); OPQ(e1411);
  SB();
  float e1412 = __builtin_amdgcn_exp2f(u141[2]); OPQ(e1412);
  SB();
  float e1413 = __builtin_amdgcn_exp2f(u141[3]); OPQ(e1413);
  SB();
  MZ(u151, asb(k1524), qg1); SB();
  unsigned y1410 = pk(e1410, e1411); OPQ(y1410);
  SB();
  unsigned y1411 = pk(e1412, e1413); OPQ(y1411);
  SB();
  const u4 p04 = {y0400, y0401, y0410, y0411};
  const u4 p14 = {y1400, y1401, y1410, y1411};
  SB();
  b8 f1526 = cat(l1526, h1526);
  MF(ol0, f1526, asb(p04)); SB();
  s4 l1530 = TR(pVL, 20480);
  SB();
  float e0500 = __builtin_amdgcn_exp2f(u050[0]); OPQ(e0500);
  SB();
  float e0501 = __builtin_amdgcn_exp2f(u050[1]); OPQ(e0501);
  SB();
  MF(ol1, f1526, asb(p14)); SB();
  s4 h1530 = TR(pVL, 21504);
  SB();
  float e0502 = __builtin_amdgcn_exp2f(u050[2]); OPQ(e0502);
  SB();
  float e0503 = __builtin_amdgcn_exp2f(u050[3]); OPQ(e0503);
  SB();
  unsigned y0500 = pk(e0500, e0501); OPQ(y0500);
  SB();
  unsigned y0501 = pk(e0502, e0503); OPQ(y0501);
  SB();
  b8 f1525 = cat(l1525, h1525);
  MF(om0, f1525, asb(p04)); SB();
  s4 l1529 = TR(pV0, 10240);
  SB();
  float e1500 = __builtin_amdgcn_exp2f(u150[0]); OPQ(e1500);
  SB();
  float e1501 = __builtin_amdgcn_exp2f(u150[1]); OPQ(e1501);
  SB();
  float e1502 = __builtin_amdgcn_exp2f(u150[2]); OPQ(e1502);
  SB();
  MF(om1, f1525, asb(p14)); SB();
  s4 h1529 = TR(pV1, 10240);
  SB();
  float e1503 = __builtin_amdgcn_exp2f(u150[3]); OPQ(e1503);
  SB();
  unsigned y1500 = pk(e1500, e1501); OPQ(y1500);
  SB();
  unsigned y1501 = pk(e1502, e1503); OPQ(y1501);
  SB();
  float e0510 = __builtin_amdgcn_exp2f(u051[0]); OPQ(e0510);
  SB();
  MZ(u060, asb(k1527), qg0); SB();
  const u4 k1531 = LR(pK_, 28672);
  SB();
  u4 h8_0, h8_1;
  { unsigned t_; asm volatile("s_mov_b32 %[t], 262144\n\ts_nop 2\n\tbuffer_load_dwordx4 %[h8_0], %[v16], %[rH], %[t] offen sc1\n\tbuffer_load_dwordx4 %[h8_1], %[v16], %[rH], %[t] offen offset:1024 sc1" : [t]"=&s"(t_), [h8_0]"=&v"(h8_0), [h8_1]"=&v"(h8_1) : [v16]"v"(v16), [rH]"s"(rH) : "memory", "scc"); }
  SB();
  SB();
  MZ(u160, asb(k1527), qg1); SB();
  float e0511 = __builtin_amdgcn_exp2f(u051[1]); OPQ(e0511);
  SB();
  float e0512 = __builtin_amdgcn_exp2f(u051[2]); OPQ(e0512);
  SB();
  float e0513 = __builtin_amdgcn_exp2f(u051[3]); OPQ(e0513);
  SB();
  unsigned y0510 = pk(e0510, e0511); OPQ(y0510);
  SB();
  unsigned y0511 = pk(e0512, e0513); OPQ(y0511);
  SB();
  float e1510 = __builtin_amdgcn_exp2f(u151[0]); OPQ(e1510);
  SB();
  MZ(u061, asb(k1528), qg0); SB();
  const u4 k1532 = LR(pK_, 29696);
  SB();
  float e1511 = __builtin_amdgcn_exp2f(u151[1]); OPQ(e1511);
  SB();
  float e1512 = __builtin_amdgcn_exp2f(u151[2]); OPQ(e1512);
  SB();
  float e1513 = __builtin_amdgcn_exp2f(u151[3]); OPQ(e1513);
  SB();
  MZ(u161, asb(k1528), qg1); SB();
  unsigned y1510 = pk(e1510, e1511); OPQ(y1510);
  SB();
  unsigned y1511 = pk(e1512, e1513); OPQ(y1511);
  SB();
  const u4 p05 = {y0500, y0501, y0510, y0511};
  const u4 p15 = {y1500, y1501, y1510, y1511};
  SB();
  b8 f1530 = cat(l1530, h1530);
  MF(ol0, f1530, asb(p05)); SB();
  s4 l1534 = TR(pVL, 24576);
  SB();
  u2 pHB;
  { asm volatile("buffer_load_dwordx2 %[pHB], %[v7], %[rWS], %[sFH] offen offset:128 sc1" : [pHB]"=&v"(pHB) : [v7]"v"(v7), [rWS]"s"(rWS), [sFH]"s"(sFH) : "memory"); }
  SB();
  MF(ol1, f1530, asb(p15)); SB();
  s4 h1534 = TR(pVL, 25600);
  SB();
  float e0600 = __builtin_amdgcn_exp2f(u060[0]); OPQ(e0600);
  SB();
  float e0601 = __builtin_amdgcn_exp2f(u060[1]); OPQ(e0601);
  SB();
  float e0602 = __builtin_amdgcn_exp2f(u060[2]); OPQ(e0602);
  SB();
  float e0603 = __builtin_amdgcn_exp2f(u060[3]); OPQ(e0603);
  SB();
  unsigned y0600 = pk(e0600, e0601); OPQ(y0600);
  SB();
  unsigned y0601 = pk(e0602, e0603); OPQ(y0601);
  SB();
  float e1600 = __builtin_amdgcn_exp2f(u160[0]); OPQ(e1600);
  SB();
  b8 f1529 = cat(l1529, h1529);
  MF(om0, f1529, asb(p05)); SB();
  s4 l1533 = TR(pV0, 12288);
  SB();
  float e1601 = __builtin_amdgcn_exp2f(u160[1]); OPQ(e1601);
  SB();
  float e1602 = __builtin_amdgcn_exp2f(u160[2]); OPQ(e1602);
  SB();
  MF(om1, f1529, asb(p15)); SB();
  s4 h1533 = TR(pV1, 12288);
  SB();
  float e1603 = __builtin_amdgcn_exp2f(u160[3]); OPQ(e1603);
  SB();
  unsigned y1600 = pk(e1600, e1601); OPQ(y1600);
  SB();
  unsigned y1601 = pk(e1602, e1603); OPQ(y1601);
  SB();
  float e0610 = __builtin_amdgcn_exp2f(u061[0]); OPQ(e0610);
  SB();
  { asm volatile("s_waitcnt vmcnt(25)\n\ts_barrier" :  :  : "memory"); }
  SB();
  MZ(u070, asb(k1531), qg0); SB();
  const u4 k1535 = LR(pK_, 32768);
  SB();
  u4 h9_0, h9_1;
  { unsigned t_; asm volatile("s_mov_b32 %[t], 278528\n\ts_nop 2\n\tbuffer_load_dwordx4 %[h9_0], %[v16], %[rH], %[t] offen sc1\n\tbuffer_load_dwordx4 %[h9_1], %[v16], %[rH], %[t] offen offset:1024 sc1" : [t]"=&s"(t_), [h9_0]"=&v"(h9_0), [h9_1]"=&v"(h9_1) : [v16]"v"(v16), [rH]"s"(rH) : "memory", "scc"); }
  SB();
  SB();
  MZ(u170, asb(k1531), qg1); SB();
  float e0611 = __builtin_amdgcn_exp2f(u061[1]); OPQ(e0611);
  SB();
  float e0612 = __builtin_amdgcn_exp2f(u061[2]); OPQ(e0612);
  SB();
  float e0613 = __builtin_amdgcn_exp2f(u061[3]); OPQ(e0613);
  SB();
  unsigned y0610 = pk(e0610, e0611); OPQ(y0610);
  SB();
  unsigned y0611 = pk(e0612, e0613); OPQ(y0611);
  SB();
  float e1610 = __builtin_amdgcn_exp2f(u161[0]); OPQ(e1610);
  SB();
  float e1611 = __builtin_amdgcn_exp2f(u161[1]); OPQ(e1611);
  SB();
  MZ(u071, asb(k1532), qg0); SB();
  const u4 k1536 = LR(pK_, 33792);
  SB();
  float e1612 = __builtin_amdgcn_exp2f(u161[2]); OPQ(e1612);
  SB();
  MZ(u171, asb(k1532), qg1); SB();
  float e1613 = __builtin_amdgcn_exp2f(u161[3]); OPQ(e1613);
  SB();
  unsigned y1610 = pk(e1610, e1611); OPQ(y1610);
  SB();
  unsigned y1611 = pk(e1612, e1613); OPQ(y1611);
  SB();
  const u4 p06 = {y0600, y0601, y0610, y0611};
  const u4 p16 = {y1600, y1601, y1610, y1611};
  SB();
  b8 f1534 = cat(l1534, h1534);
  MF(ol0, f1534, asb(p06)); SB();
  s4 l1538 = TR(pVL, 28672);
  SB();
  { unsigned t_; asm volatile("s_add_u32 m0, %[wb], 0\n\ts_add_u32 %[t], %[sX1], 147456\n\tbuffer_load_dwordx4 %[vW2], %[rW2X], %[t] offen lds" : [t]"=&s"(t_) : [wb]"s"(wb), [sX1]"s"(sX1), [vW2]"v"(vW2), [rW2X]"s"(rW2X) : "memory", "scc"); }
  SB();
  MF(ol1, f1534, asb(p16)); SB();
  s4 h1538 = TR(pVL, 29696);
  SB();
  float e0700 = __builtin_amdgcn_exp2f(u070[0]); OPQ(e0700);
  SB();
  float e0701 = __builtin_amdgcn_exp2f(u070[1]); OPQ(e0701);
  SB();
  float e0702 = __builtin_amdgcn_exp2f(u070[2]); OPQ(e0702);
  SB();
  float e0703 = __builtin_amdgcn_exp2f(u070[3]); OPQ(e0703);
  SB();
  unsigned y0700 = pk(e0700, e0701); OPQ(y0700);
  SB();
  unsigned y0701 = pk(e0702, e0703); OPQ(y0701);
  SB();
  float e1700 = __builtin_amdgcn_exp2f(u170[0]); OPQ(e1700);
  SB();
  float e1701 = __builtin_amdgcn_exp2f(u170[1]); OPQ(e1701);
  SB();
  b8 f1533 = cat(l1533, h1533);
  MF(om0, f1533, asb(p06)); SB();
  s4 l1537 = TR(pV0, 14336);
  SB();
  float e1702 = __builtin_amdgcn_exp2f(u170[2]); OPQ(e1702);
  SB();
  float e1703 = __builtin_amdgcn_exp2f(u170[3]); OPQ(e1703);
  SB();
  unsigned y1700 = pk(e1700, e1701); OPQ(y1700);
  SB();
  unsigned y1701 = pk(e1702, e1703); OPQ(y1701);
  SB();
  MF(om1, f1533, asb(p16)); SB();
  s4 h1537 = TR(pV1, 14336);
  SB();
  { unsigned t_; asm volatile("s_add_u32 m0, %[wb], 49152\n\ts_add_u32 %[t], %[sX2], 147456\n\tbuffer_load_dwordx4 %[vW2], %[rW2X], %[t] offen lds" : [t]"=&s"(t_) : [wb]"s"(wb), [sX2]"s"(sX2), [vW2]"v"(vW2), [rW2X]"s"(rW2X) : "memory", "scc"); }
  SB();
  MI(u080, asb(k1535), qg0, cd8); SB();
  s4 l1540 = TR(pVL, 32768);
  SB();
  { asm volatile("s_waitcnt vmcnt(4)" : [pHB]"+v"(pHB) :  : "memory"); }
  repoll<128>(pHB, rWS, v7, sFH, T_); SB();
  SB();
  MZ(u180, asb(k1535), qg1); SB();
  s4 h1540 = TR(pVL, 33792);
  SB();
  float e0710 = __builtin_amdgcn_exp2f(u071[0]); OPQ(e0710);
  SB();
  float e0711 = __builtin_amdgcn_exp2f(u071[1]); OPQ(e0711);
  SB();
  float e0712 = __builtin_amdgcn_exp2f(u071[2]); OPQ(e0712);
  SB();
  float e0713 = __builtin_amdgcn_exp2f(u071[3]); OPQ(e0713);
  SB();
  unsigned y0710 = pk(e0710, e0711); OPQ(y0710);
  SB();
  unsigned y0711 = pk(e0712, e0713); OPQ(y0711);
  SB();
  float e1710 = __builtin_amdgcn_exp2f(u171[0]); OPQ(e1710);
  SB();
  float e1711 = __builtin_amdgcn_exp2f(u171[1]); OPQ(e1711);
  SB();
  float e1712 = __builtin_amdgcn_exp2f(u171[2]); OPQ(e1712);
  SB();
  float e1713 = __builtin_amdgcn_exp2f(u171[3]); OPQ(e1713);
  SB();
  unsigned y1710 = pk(e1710, e1711); OPQ(y1710);
  SB();
  unsigned y1711 = pk(e1712, e1713); OPQ(y1711);
  SB();
  MI(u181, asb(k1536), qg1, cd8); SB();
  s4 l1539 = TR(pV0, 16384);
  s4 h1539 = TR(pV1, 16384);
  SB();
  u4 h11_0, h11_1;
  { unsigned t_; asm volatile("s_xor_b32 %[t], %[sJ], 98304\n\ts_nop 2\n\tbuffer_load_dwordx4 %[h11_0], %[v16], %[rH], %[t] offen sc1\n\tbuffer_load_dwordx4 %[h11_1], %[v16], %[rH], %[t] offen offset:1024 sc1" : [t]"=&s"(t_), [h11_0]"=&v"(h11_0), [h11_1]"=&v"(h11_1) : [sJ]"s"(sJ), [v16]"v"(v16), [rH]"s"(rH) : "memory", "scc"); }
  SB();
  SB();
  const u4 p07 = {y0700, y0701, y0710, y0711};
  const u4 p17 = {y1700, y1701, y1710, y1711};
  SB();
  b8 f1538 = cat(l1538, h1538);
  MF(ol0, f1538, asb(p07)); SB();
  s4 l1235 = TR(pA0, 57344);
  SB();
  float e0800 = __builtin_amdgcn_exp2f(u080[0]); OPQ(e0800);
  SB();
  float e0801 = __builtin_amdgcn_exp2f(u080[1]); OPQ(e0801);
  SB();
  float e0802 = __builtin_amdgcn_exp2f(u080[2]); OPQ(e0802);
  SB();
  float e0803 = __builtin_amdgcn_exp2f(u080[3]); OPQ(e0803);
  SB();
  unsigned y0800 = pk(e0800, e0801); OPQ(y0800);
  SB();
  unsigned y0801 = pk(e0802, e0803); OPQ(y0801);
  SB();
  float e1800 = __builtin_amdgcn_exp2f(u180[0]); OPQ(e1800);
  SB();
  float e1801 = __builtin_amdgcn_exp2f(u180[1]); OPQ(e1801);
  SB();
  float e1802 = __builtin_amdgcn_exp2f(u180[2]); OPQ(e1802);
  SB();
  float e1803 = __builtin_amdgcn_exp2f(u180[3]); OPQ(e1803);
  SB();
  unsigned y1800 = pk(e1800, e1801); OPQ(y1800);
  SB();
  MF(ol1, f1538, asb(p17)); SB();
  s4 h1235 = TR(pA0, 59392);
  SB();
  unsigned y1801 = pk(e1802, e1803); OPQ(y1801);
  SB();
  const unsigned y0810 = zz(), y0811 = zz();
  SB();
  b8 f1537 = cat(l1537, h1537);
  MF(om0, f1537, asb(p07)); SB();
  s4 l1236 = TR(pA1, 57344);
  SB();
  float e1810 = __builtin_amdgcn_exp2f(u181[0]); OPQ(e1810);
  SB();
  float e1811 = __builtin_amdgcn_exp2f(u181[1]); OPQ(e1811);
  SB();
  float e1812 = __builtin_amdgcn_exp2f(u181[2]); OPQ(e1812);
  SB();
  MF(om1, f1537, asb(p17)); SB();
  s4 h1236 = TR(pA1, 59392);
  SB();
  u4 h12_0, h12_1;
  { unsigned t_; asm volatile("s_xor_b32 %[t], %[sJ], 163840\n\ts_nop 2\n\tbuffer_load_dwordx4 %[h12_0], %[v16], %[rH], %[t] offen sc1\n\tbuffer_load_dwordx4 %[h12_1], %[v16], %[rH], %[t] offen offset:1024 sc1" : [t]"=&s"(t_), [h12_0]"=&v"(h12_0), [h12_1]"=&v"(h12_1) : [sJ]"s"(sJ), [v16]"v"(v16), [rH]"s"(rH) : "memory", "scc"); }
  SB();
  SB();
  float e1813 = __builtin_amdgcn_exp2f(u181[3]); OPQ(e1813);
  SB();
  unsigned y1810 = pk(e1810, e1811); OPQ(y1810);
  SB();
  unsigned y1811 = pk(e1812, e1813); OPQ(y1811);
  SB();
  const u4 p08 = {y0800, y0801, y0810, y0811};
  const u4 p18 = {y1800, y1801, y1810, y1811};
  SB();
  b8 f1540 = cat(l1540, h1540);
  MF(ol0, f1540, asb(p08)); SB();
  s4 l1237 = TR(pA2, 57344);
  SB();
  float Ga0 = __builtin_fmaxf(rb(1.f - gt0), 0x1p-60f); OPQ(Ga0);
  SB();
  float Ga1 = __builtin_fmaxf(rb(1.f - gt1), 0x1p-60f); OPQ(Ga1);
  SB();
  MF(ol1, f1540, asb(p18)); SB();
  s4 h1237 = TR(pA2, 59392);
  SB();
  b8 f1539 = cat(l1539, h1539);
  MF(om0, f1539, asb(p08)); SB();
  s4 l1238 = TR(pA3, 57344);
  SB();
  MF(om1, f1539, asb(p18)); SB();
  s4 h1238 = TR(pA3, 59392);
  SB();
  { unsigned t_; asm volatile("s_add_u32 m0, %[wb], 8192\n\ts_add_u32 %[t], %[sX3], 147456\n\tbuffer_load_dwordx4 %[vW2], %[rW2X], %[t] offen lds" : [t]"=&s"(t_) : [wb]"s"(wb), [sX3]"s"(sX3), [vW2]"v"(vW2), [rW2X]"s"(rW2X) : "memory", "scc"); }
  SB();
  const b8 H0_0 = asb(hk00), H0_1 = asb(hk01);
  SB();
  SB();
  SB();
  b8 f1235 = cat(l1235, h1235);
  MZ(dX00, f1235, H0_0); SB();
  s4 l1239 = TR(pA0, 16384);
  SB();
  MZ(dX01, f1235, H0_1); SB();
  s4 h1239 = TR(pA0, 18432);
  SB();
  b8 f1236 = cat(l1236, h1236);
  MZ(dX10, f1236, H0_0); SB();
  s4 l1240 = TR(pA1, 16384);
  SB();
  float os0 = __builtin_amdgcn_rcpf(ol0[2] * Ga0); OPQ(os0);
  SB();
  float os1 = __builtin_amdgcn_rcpf(ol1[2] * Ga1); OPQ(os1);
  SB();
  MZ(dX11, f1236, H0_1); SB();
  s4 h1240 = TR(pA1, 18432);
  SB();
  os0 *= gt0; OPQ(os0); SB(); os1 *= gt1; OPQ(os1); SB(); u2 oa_, ob_;
  SB();
  b8 f1237 = cat(l1237, h1237);
  MZ(dX20, f1237, H0_0); SB();
  s4 l1241 = TR(pA2, 16384);
  SB();
  u4 h13_0, h13_1;
  { unsigned t_; asm volatile("s_xor_b32 %[t], %[sJ], 229376\n\ts_nop 2\n\tbuffer_load_dwordx4 %[h13_0], %[v16], %[rH], %[t] offen sc1\n\tbuffer_load_dwordx4 %[h13_1], %[v16], %[rH], %[t] offen offset:1024 sc1" : [t]"=&s"(t_), [h13_0]"=&v"(h13_0), [h13_1]"=&v"(h13_1) : [sJ]"s"(sJ), [v16]"v"(v16), [rH]"s"(rH) : "memory", "scc"); }
  SB();
  SB();
  MZ(dX21, f1237, H0_1); SB();
  s4 h1241 = TR(pA2, 18432);
  SB();
  oa_[0] = ({ float a_ = om0[0] * os0; OPQ(a_); SB(); float b_ = om0[1] * os0; pk(a_, b_); });
  SB();
  oa_[1] = ({ float a_ = om0[2] * os0; OPQ(a_); SB(); float b_ = om0[3] * os0; pk(a_, b_); }); OPQ(oa_);
  SB();
  b8 f1238 = cat(l1238, h1238);
  MZ(dX30, f1238, H0_0); SB();
  s4 l1242 = TR(pA3, 16384);
  SB();
  ob_[0] = ({ float a_ = om1[0] * os1; OPQ(a_); SB(); float b_ = om1[1] * os1; pk(a_, b_); });
  SB();
  MZ(dX31, f1238, H0_1); SB();
  s4 h1242 = TR(pA3, 18432);
  SB();
  ob_[1] = ({ float a_ = om1[2] * os1; OPQ(a_); SB(); float b_ = om1[3] * os1; pk(a_, b_); }); OPQ(ob_);
  SB();
  { asm volatile("s_waitcnt vmcnt(25)" : [h1_0]"+v"(h1_0), [h1_1]"+v"(h1_1) :  : "memory"); }
  const b8 H1_0 = asb(h1_0), H1_1 = asb(h1_1);
  SB();
  b8 f1239 = cat(l1239, h1239);
  MA(dX00, f1239, H1_0); SB();
  s4 l1243 = TR(pA0, 32768);
  SB();
  st16((j >> 1) ? cat2(ob_, oa_) : cat2(oa_, ob_), rWS, v16, 14680064u + (((Rb * 16 + c) * 4 + r) * 2 + cg) * 1024);
  SB();
  MA(dX01, f1239, H1_1); SB();
  s4 h1243 = TR(pA0, 34816);
  SB();
  b8 f1240 = cat(l1240, h1240);
  MA(dX10, f1240, H1_0); SB();
  s4 l1244 = TR(pA1, 32768);
  SB();
  unsigned oc_ = ({ float a_ = ol0[0] * os0; OPQ(a_); SB(); float b_ = ol0[1] * os0; pk(a_, b_); }); OPQ(oc_);
  SB();
  MA(dX11, f1240, H1_1); SB();
  s4 h1244 = TR(pA1, 34816);
  SB();
  b8 f1241 = cat(l1241, h1241);
  MA(dX20, f1241, H1_0); SB();
  s4 l1245 = TR(pA2, 32768);
  SB();
  u2 pHC;
  { asm volatile("buffer_load_dwordx2 %[pHC], %[v7], %[rWS], %[sFH] offen offset:256 sc1" : [pHC]"=&v"(pHC) : [v7]"v"(v7), [rWS]"s"(rWS), [sFH]"s"(sFH) : "memory"); }
  SB();
  MA(dX21, f1241, H1_1); SB();
  s4 h1245 = TR(pA2, 34816);
  SB();
  unsigned od_ = ({ float a_ = ol1[0] * os1; OPQ(a_); SB(); float b_ = ol1[1] * os1; pk(a_, b_); }); OPQ(od_);
  SB();
  { const unsigned vq_ = LN(0xffffull) ? v16 >> 1 : vF; st8((u2){oc_, od_}, rWS, vq_, 16777216u + ((Rb * 4 + gi) * 4 + r) * 1024 + cg * 512 + j * 128); }
  SB();
  b8 f1242 = cat(l1242, h1242);
  MA(dX30, f1242, H1_0); SB();
  s4 l1246 = TR(pA3, 32768);
  SB();
  float G0 = cg ? Ga1 : Ga0; OPQ(G0); SB(); float G1 = cg ? Ga0 : Ga1; OPQ(G1);
  SB();
  MA(dX31, f1242, H1_1); SB();
  s4 h1246 = TR(pA3, 34816);
  SB();
  { asm volatile("s_waitcnt vmcnt(26)" : [h2_0]"+v"(h2_0), [h2_1]"+v"(h2_1) :  : "memory"); }
  const b8 H2_0 = asb(h2_0), H2_1 = asb(h2_1);
  SB();
  b8 f1243 = cat(l1243, h1243);
  MA(dX00, f1243, H2_0); SB();
  s4 l1247 = TR(pA0, 24576);
  SB();
  MA(dX01, f1243, H2_1); SB();
  s4 h1247 = TR(pA0, 26624);
  SB();
  b8 f1244 = cat(l1244, h1244);
  MA(dX10, f1244, H2_0); SB();
  s4 l1248 = TR(pA1, 24576);
  SB();
  MA(dX11, f1244, H2_1); SB();
  s4 h1248 = TR(pA1, 26624);
  SB();
  b8 f1245 = cat(l1245, h1245);
  MA(dX20, f1245, H2_0); SB();
  s4 l1249 = TR(pA2, 24576);
  SB();
  MA(dX21, f1245, H2_1); SB();
  s4 h1249 = TR(pA2, 26624);
  SB();
  b8 f1246 = cat(l1246, h1246);
  MA(dX30, f1246, H2_0); SB();
  s4 l1250 = TR(pA3, 24576);
  SB();
  MA(dX31, f1246, H2_1); SB();
  s4 h1250 = TR(pA3, 26624);
  SB();
  { asm volatile("s_waitcnt vmcnt(24)" : [h3_0]"+v"(h3_0), [h3_1]"+v"(h3_1) :  : "memory"); }
  const b8 H3_0 = asb(h3_0), H3_1 = asb(h3_1);
  SB();
  b8 f1247 = cat(l1247, h1247);
  MA(dX00, f1247, H3_0); SB();
  s4 l1251 = TR(pA0, 40960);
  SB();
  { asm volatile("s_waitcnt vmcnt(1)" : [pHC]"+v"(pHC) :  : "memory"); }
  repoll<256>(pHC, rWS, v7, sFH, T_); SB();
  SB();
  MA(dX01, f1247, H3_1); SB();
  s4 h1251 = TR(pA0, 43008);
  SB();
  u4 h15_0, h15_1;
  { unsigned t_; asm volatile("s_xor_b32 %[t], %[sJ], 114688\n\ts_nop 2\n\tbuffer_load_dwordx4 %[h15_0], %[v16], %[rH], %[t] offen sc1\n\tbuffer_load_dwordx4 %[h15_1], %[v16], %[rH], %[t] offen offset:1024 sc1" : [t]"=&s"(t_), [h15_0]"=&v"(h15_0), [h15_1]"=&v"(h15_1) : [sJ]"s"(sJ), [v16]"v"(v16), [rH]"s"(rH) : "memory", "scc"); }
  SB();
  SB();
  b8 f1248 = cat(l1248, h1248);
  MA(dX10, f1248, H3_0); SB();
  s4 l1252 = TR(pA1, 40960);
  SB();
  MA(dX11, f1248, H3_1); SB();
  s4 h1252 = TR(pA1, 43008);
  SB();
  b8 f1249 = cat(l1249, h1249);
  MA(dX20, f1249, H3_0); SB();
  s4 l1253 = TR(pA2, 40960);
  SB();
  MA(dX21, f1249, H3_1); SB();
  s4 h1253 = TR(pA2, 43008);
  SB();
  b8 f1250 = cat(l1250, h1250);
  MA(dX30, f1250, H3_0); SB();
  s4 l1254 = TR(pA3, 40960);
  SB();
  MA(dX31, f1250, H3_1); SB();
  s4 h1254 = TR(pA3, 43008);
  SB();
  const b8 H4_0 = asb(hk10), H4_1 = asb(hk11);
  SB();
  b8 f1251 = cat(l1251, h1251);
  MA(dX00, f1251, H4_0); SB();
  s4 l1255 = TR(pA0, 49152);
  SB();
  u4 h16_0, h16_1;
  { unsigned t_; asm volatile("s_xor_b32 %[t], %[sJ], 180224\n\ts_nop 2\n\tbuffer_load_dwordx4 %[h16_0], %[v16], %[rH], %[t] offen sc1\n\tbuffer_load_dwordx4 %[h16_1], %[v16], %[rH], %[t] offen offset:1024 sc1" : [t]"=&s"(t_), [h16_0]"=&v"(h16_0), [h16_1]"=&v"(h16_1) : [sJ]"s"(sJ), [v16]"v"(v16), [rH]"s"(rH) : "memory", "scc"); }
  SB();
  SB();
  MA(dX01, f1251, H4_1); SB();
  s4 h1255 = TR(pA0, 51200);
  SB();
  b8 f1252 = cat(l1252, h1252);
  MA(dX10, f1252, H4_0); SB();
  s4 l1256 = TR(pA1, 49152);
  SB();
  MA(dX11, f1252, H4_1); SB();
  s4 h1256 = TR(pA1, 51200);
  SB();
  b8 f1253 = cat(l1253, h1253);
  MA(dX20, f1253, H4_0); SB();
  s4 l1257 = TR(pA2, 49152);
  SB();
  MA(dX21, f1253, H4_1); SB();
  s4 h1257 = TR(pA2, 51200);
  SB();
  b8 f1254 = cat(l1254, h1254);
  MA(dX30, f1254, H4_0); SB();
  s4 l1258 = TR(pA3, 49152);
  SB();
  { asm volatile("s_waitcnt vmcnt(4)" :  :  : "memory"); }
  st8((u2){tlo, thi}, rWS, vF + 384, sGH); SB();
  SB();
  MA(dX31, f1254, H4_1); SB();
  s4 h1258 = TR(pA3, 51200);
  SB();
  { asm volatile("s_waitcnt vmcnt(27)" : [h5_0]"+v"(h5_0), [h5_1]"+v"(h5_1) :  : "memory"); }
  const b8 H5_0 = asb(h5_0), H5_1 = asb(h5_1);
  SB();
  b8 f1255 = cat(l1255, h1255);
  MA(dX00, f1255, H5_0); SB();
  pA0 += 40960u; OPQ(pA0);
  s4 l1259 = TR(pA0, 49152);
  SB();
  u4 h17_0, h17_1;
  { unsigned t_; asm volatile("s_xor_b32 %[t], %[sJ], 245760\n\ts_nop 2\n\tbuffer_load_dwordx4 %[h17_0], %[v16], %[rH], %[t] offen sc1\n\tbuffer_load_dwordx4 %[h17_1], %[v16], %[rH], %[t] offen offset:1024 sc1" : [t]"=&s"(t_), [h17_0]"=&v"(h17_0), [h17_1]"=&v"(h17_1) : [sJ]"s"(sJ), [v16]"v"(v16), [rH]"s"(rH) : "memory", "scc"); }
  SB();
  SB();
  MA(dX01, f1255, H5_1); SB();
  s4 h1259 = TR(pA0, 51200);
  SB();
  b8 f1256 = cat(l1256, h1256);
  MA(dX10, f1256, H5_0); SB();
  pA1 += 40960u; OPQ(pA1);
  s4 l1260 = TR(pA1, 49152);
  SB();
  MA(dX11, f1256, H5_1); SB();
  s4 h1260 = TR(pA1, 51200);
  SB();
  b8 f1257 = cat(l1257, h1257);
  MA(dX20, f1257, H5_0); SB();
  pA2 += 40960u; OPQ(pA2);
  s4 l1261 = TR(pA2, 49152);
  SB();
  MA(dX21, f1257, H5_1); SB();
  s4 h1261 = TR(pA2, 51200);
  SB();
  b8 f1258 = cat(l1258, h1258);
  MA(dX30, f1258, H5_0); SB();
  pA3 += 40960u; OPQ(pA3);
  s4 l1262 = TR(pA3, 49152);
  SB();
  MA(dX31, f1258, H5_1); SB();
  s4 h1262 = TR(pA3, 51200);
  SB();
  { asm volatile("s_waitcnt vmcnt(12)\n\ts_barrier" :  :  : "memory"); }
  SB();
  { asm volatile("s_waitcnt vmcnt(26)" : [h6_0]"+v"(h6_0), [h6_1]"+v"(h6_1) :  : "memory"); }
  const b8 H6_0 = asb(h6_0), H6_1 = asb(h6_1);
  SB();
  b8 f1259 = cat(l1259, h1259);
  MA(dX00, f1259, H6_0); SB();
  s4 l1263 = TR(pA0, 57344);
  SB();
  { unsigned t_; asm volatile("s_add_u32 m0, %[wb], 24576\n\ts_add_u32 %[t], %[sX0], 221184\n\tbuffer_load_dwordx4 %[vW2], %[rW2X], %[t] offen lds" : [t]"=&s"(t_) : [wb]"s"(wb), [sX0]"s"(sX0), [vW2]"v"(vW2), [rW2X]"s"(rW2X) : "memory", "scc"); }
  SB();
  MA(dX01, f1259, H6_1); SB();
  s4 h1263 = TR(pA0, 59392);
  SB();
  b8 f1260 = cat(l1260, h1260);
  MA(dX10, f1260, H6_0); SB();
  s4 l1264 = TR(pA1, 57344);
  SB();
  MA(dX11, f1260, H6_1); SB();
  s4 h1264 = TR(pA1, 59392);
  SB();
  b8 f1261 = cat(l1261, h1261);
  MA(dX20, f1261, H6_0); SB();
  s4 l1265 = TR(pA2, 57344);
  SB();
  MA(dX21, f1261, H6_1); SB();
  s4 h1265 = TR(pA2, 59392);
  SB();
  { unsigned t_; asm volatile("s_add_u32 m0, %[wb], 32768\n\ts_add_u32 %[t], %[sX1], 221184\n\tbuffer_load_dwordx4 %[vW2], %[rW2X], %[t] offen lds" : [t]"=&s"(t_) : [wb]"s"(wb), [sX1]"s"(sX1), [vW2]"v"(vW2), [rW2X]"s"(rW2X) : "memory", "scc"); }
  SB();
  b8 f1262 = cat(l1262, h1262);
  MA(dX30, f1262, H6_0); SB();
  s4 l1266 = TR(pA3, 57344);
  SB();
  MA(dX31, f1262, H6_1); SB();
  s4 h1266 = TR(pA3, 59392);
  SB();
  { asm volatile("s_waitcnt vmcnt(26)" : [h7_0]"+v"(h7_0), [h7_1]"+v"(h7_1) :  : "memory"); }
  const b8 H7_0 = asb(h7_0), H7_1 = asb(h7_1);
  SB();
  b8 f1263 = cat(l1263, h1263);
  MA(dX00, f1263, H7_0); SB();
  s4 l1267 = TR(pA0, 32768);
  SB();
  MA(dX01, f1263, H7_1); SB();
  s4 h1267 = TR(pA0, 34816);
  SB();
  b8 f1264 = cat(l1264, h1264);
  MA(dX10, f1264, H7_0); SB();
  s4 l1268 = TR(pA1, 32768);
  SB();
  { unsigned t_; asm volatile("s_add_u32 m0, %[wb], 16384\n\ts_add_u32 %[t], %[sX2], 221184\n\tbuffer_load_dwordx4 %[vW2], %[rW2X], %[t] offen lds" : [t]"=&s"(t_) : [wb]"s"(wb), [sX2]"s"(sX2), [vW2]"v"(vW2), [rW2X]"s"(rW2X) : "memory", "scc"); }
  SB();
  MA(dX11, f1264, H7_1); SB();
  s4 h1268 = TR(pA1, 34816);
  SB();
  b8 f1265 = cat(l1265, h1265);
  MA(dX20, f1265, H7_0); SB();
  s4 l1269 = TR(pA2, 32768);
  SB();
  MA(dX21, f1265, H7_1); SB();
  s4 h1269 = TR(pA2, 34816);
  SB();
  b8 f1266 = cat(l1266, h1266);
  MA(dX30, f1266, H7_0); SB();
  s4 l1270 = TR(pA3, 32768);
  SB();
  MA(dX31, f1266, H7_1); SB();
  s4 h1270 = TR(pA3, 34816);
  SB();
  { unsigned t_; asm volatile("s_add_u32 m0, %[wb], 65536\n\ts_add_u32 %[t], %[sX3], 221184\n\tbuffer_load_dwordx4 %[vW2], %[rW2X], %[t] offen lds" : [t]"=&s"(t_) : [wb]"s"(wb), [sX3]"s"(sX3), [vW2]"v"(vW2), [rW2X]"s"(rW2X) : "memory", "scc"); }
  SB();
  { asm volatile("s_waitcnt vmcnt(26)" : [h8_0]"+v"(h8_0), [h8_1]"+v"(h8_1) :  : "memory"); }
  const b8 H8_0 = asb((u4){h8_0[0], h8_0[1], h8_1[0], h8_1[1]}), H8_1 = asb((u4){h8_0[2], h8_0[3], h8_1[2], h8_1[3]});
  SB();
  b8 f1267 = cat(l1267, h1267);
  MA(dX00, f1267, H8_0); SB();
  s4 l1271 = TR(pA0, 40960);
  SB();
  MA(dX01, f1267, H8_1); SB();
  s4 h1271 = TR(pA0, 43008);
  SB();
  b8 f1268 = cat(l1268, h1268);
  MA(dX10, f1268, H8_0); SB();
  s4 l1272 = TR(pA1, 40960);
  SB();
  MA(dX11, f1268, H8_1); SB();
  s4 h1272 = TR(pA1, 43008);
  SB();
  b8 f1269 = cat(l1269, h1269);
  MA(dX20, f1269, H8_0); SB();
  s4 l1273 = TR(pA2, 40960);
  SB();
  { unsigned t_; asm volatile("s_add_u32 m0, %[wb], 40960\n\ts_mov_b32 %[t], 204800\n\tbuffer_load_dwordx4 %[vL], %[rPK], %[t] offen sc1 lds" : [t]"=&s"(t_) : [wb]"s"(wb), [vL]"v"(vL), [rPK]"s"(rPK) : "memory", "scc"); }
  SB();
  MA(dX21, f1269, H8_1); SB();
  s4 h1273 = TR(pA2, 43008);
  SB();
  b8 f1270 = cat(l1270, h1270);
  MA(dX30, f1270, H8_0); SB();
  s4 l1274 = TR(pA3, 40960);
  SB();
  MA(dX31, f1270, H8_1); SB();
  s4 h1274 = TR(pA3, 43008);
  SB();
  { asm volatile("s_waitcnt vmcnt(24)" : [h9_0]"+v"(h9_0), [h9_1]"+v"(h9_1) :  : "memory"); }
  const b8 H9_0 = asb((u4){h9_0[0], h9_0[1], h9_1[0], h9_1[1]}), H9_1 = asb((u4){h9_0[2], h9_0[3], h9_1[2], h9_1[3]});
  SB();
  b8 f1271 = cat(l1271, h1271);
  MA(dX00, f1271, H9_0); SB();
  s4 l1275 = TR(pA0, 24576);
  SB();
  MA(dX01, f1271, H9_1); SB();
  s4 h1275 = TR(pA0, 26624);
  SB();
  { unsigned t_; asm volatile("s_add_u32 m0, %[wb], 73728\n\ts_mov_b32 %[t], 212992\n\tbuffer_load_dwordx4 %[vL], %[rPK], %[t] offen sc1 lds" : [t]"=&s"(t_) : [wb]"s"(wb), [vL]"v"(vL), [rPK]"s"(rPK) : "memory", "scc"); }
  SB();
  b8 f1272 = cat(l1272, h1272);
  MA(dX10, f1272, H9_0); SB();
  s4 l1276 = TR(pA1, 24576);
  SB();
  MA(dX11, f1272, H9_1); SB();
  s4 h1276 = TR(pA1, 26624);
  SB();
  b8 f1273 = cat(l1273, h1273);
  MA(dX20, f1273, H9_0); SB();
  s4 l1277 = TR(pA2, 24576);
  SB();
  MA(dX21, f1273, H9_1); SB();
  s4 h1277 = TR(pA2, 26624);
  SB();
  b8 f1274 = cat(l1274, h1274);
  MA(dX30, f1274, H9_0); SB();
  s4 l1278 = TR(pA3, 24576);
  SB();
  { unsigned t_; asm volatile("s_add_u32 m0, %[wb], 81920\n\ts_mov_b32 %[t], 221184\n\tbuffer_load_dwordx4 %[vL], %[rPK], %[t] offen sc1 lds" : [t]"=&s"(t_) : [wb]"s"(wb), [vL]"v"(vL), [rPK]"s"(rPK) : "memory", "scc"); }
  SB();
  MA(dX31, f1274, H9_1); SB();
  s4 h1278 = TR(pA3, 26624);
  SB();
  const b8 H10_0 = asb(hk20), H10_1 = asb(hk21);
  SB();
  b8 f1275 = cat(l1275, h1275);
  MA(dX00, f1275, H10_0); SB();
  pA0 += 4294868992u; OPQ(pA0);
  s4 l1279 = TR(pA0, 0);
  SB();
  MA(dX01, f1275, H10_1); SB();
  s4 h1279 = TR(pA0, 2048);
  SB();
  b8 f1276 = cat(l1276, h1276);
  MA(dX10, f1276, H10_0); SB();
  pA1 += 4294868992u; OPQ(pA1);
  s4 l1280 = TR(pA1, 0);
  SB();
  MA(dX11, f1276, H10_1); SB();
  s4 h1280 = TR(pA1, 2048);
  SB();
  { unsigned t_; asm volatile("s_add_u32 m0, %[wb], 114688\n\ts_mov_b32 %[t], 0\n\tbuffer_load_dwordx4 %[vL], %[rPK], %[t] offen sc1 lds" : [t]"=&s"(t_) : [wb]"s"(wb), [vL]"v"(vL), [rPK]"s"(rPK) : "memory", "scc"); }
  SB();
  b8 f1277 = cat(l1277, h1277);
  MA(dX20, f1277, H10_0); SB();
  pA2 += 4294868992u; OPQ(pA2);
  s4 l1281 = TR(pA2, 0);
  SB();
  MA(dX21, f1277, H10_1); SB();
  s4 h1281 = TR(pA2, 2048);
  SB();
  b8 f1278 = cat(l1278, h1278);
  MA(dX30, f1278, H10_0); SB();
  pA3 += 4294868992u; OPQ(pA3);
  s4 l1282 = TR(pA3, 0);
  SB();
  MA(dX31, f1278, H10_1); SB();
  s4 h1282 = TR(pA3, 2048);
  SB();
  { asm volatile("s_waitcnt vmcnt(23)" : [h11_0]"+v"(h11_0), [h11_1]"+v"(h11_1) :  : "memory"); }
  const b8 H11_0 = asb(h11_0), H11_1 = asb(h11_1);
  SB();
  b8 f1279 = cat(l1279, h1279);
  MA(dX00, f1279, H11_0); SB();
  s4 l1283 = TR(pA0, 49152);
  SB();
  { unsigned t_; asm volatile("s_add_u32 m0, %[wb], 57344\n\ts_mov_b32 %[t], 8192\n\tbuffer_load_dwordx4 %[vL], %[rPK], %[t] offen sc1 lds" : [t]"=&s"(t_) : [wb]"s"(wb), [vL]"v"(vL), [rPK]"s"(rPK) : "memory", "scc"); }
  SB();
  MA(dX01, f1279, H11_1); SB();
  s4 h1283 = TR(pA0, 51200);
  SB();
  u2 pO;
  { asm volatile("buffer_load_dwordx2 %[pO], %[v7], %[rWS], %[sFH] offen offset:384 sc1" : [pO]"=&v"(pO) : [v7]"v"(v7), [rWS]"s"(rWS), [sFH]"s"(sFH) : "memory"); }
  SB();
  b8 f1280 = cat(l1280, h1280);
  MA(dX10, f1280, H11_0); SB();
  s4 l1284 = TR(pA1, 49152);
  SB();
  MA(dX11, f1280, H11_1); SB();
  s4 h1284 = TR(pA1, 51200);
  SB();
  b8 f1281 = cat(l1281, h1281);
  MA(dX20, f1281, H11_0); SB();
  s4 l1285 = TR(pA2, 49152);
  SB();
  MA(dX21, f1281, H11_1); SB();
  s4 h1285 = TR(pA2, 51200);
  SB();
  { unsigned t_; asm volatile("s_add_u32 m0, %[wb], 90112\n\ts_mov_b32 %[t], 16384\n\tbuffer_load_dwordx4 %[vL], %[rPK], %[t] offen sc1 lds" : [t]"=&s"(t_) : [wb]"s"(wb), [vL]"v"(vL), [rPK]"s"(rPK) : "memory", "scc"); }
  SB();
  b8 f1282 = cat(l1282, h1282);
  MA(dX30, f1282, H11_0); SB();
  s4 l1286 = TR(pA3, 49152);
  SB();
  MA(dX31, f1282, H11_1); SB();
  s4 h1286 = TR(pA3, 51200);
  SB();
  { asm volatile("s_waitcnt vmcnt(24)" : [h12_0]"+v"(h12_0), [h12_1]"+v"(h12_1) :  : "memory"); }
  const b8 H12_0 = asb(h12_0), H12_1 = asb(h12_1);
  SB();
  b8 f1283 = cat(l1283, h1283);
  MA(dX00, f1283, H12_0); SB();
  s4 l1287 = TR(pA0, 8192);
  SB();
  MA(dX01, f1283, H12_1); SB();
  s4 h1287 = TR(pA0, 10240);
  SB();
  b8 f1284 = cat(l1284, h1284);
  MA(dX10, f1284, H12_0); SB();
  s4 l1288 = TR(pA1, 8192);
  SB();
  { unsigned t_; asm volatile("s_add_u32 m0, %[wb], 106496\n\ts_mov_b32 %[t], 24576\n\tbuffer_load_dwordx4 %[vL], %[rPK], %[t] offen sc1 lds" : [t]"=&s"(t_) : [wb]"s"(wb), [vL]"v"(vL), [rPK]"s"(rPK) : "memory", "scc"); }
  SB();
  MA(dX11, f1284, H12_1); SB();
  s4 h1288 = TR(pA1, 10240);
  SB();
  b8 f1285 = cat(l1285, h1285);
  MA(dX20, f1285, H12_0); SB();
  s4 l1289 = TR(pA2, 8192);
  SB();
  MA(dX21, f1285, H12_1); SB();
  s4 h1289 = TR(pA2, 10240);
  SB();
  b8 f1286 = cat(l1286, h1286);
  MA(dX30, f1286, H12_0); SB();
  s4 l1290 = TR(pA3, 8192);
  SB();
  MA(dX31, f1286, H12_1); SB();
  s4 h1290 = TR(pA3, 10240);
  SB();
  { unsigned t_; asm volatile("s_add_u32 m0, %[wb], 98304\n\ts_mov_b32 %[t], 32768\n\tbuffer_load_dwordx4 %[vL], %[rPK], %[t] offen sc1 lds" : [t]"=&s"(t_) : [wb]"s"(wb), [vL]"v"(vL), [rPK]"s"(rPK) : "memory", "scc"); }
  SB();
  { asm volatile("s_waitcnt vmcnt(6)\n\ts_barrier" :  :  : "memory"); }
  SB();
  { asm volatile("s_waitcnt vmcnt(23)" : [h13_0]"+v"(h13_0), [h13_1]"+v"(h13_1) :  : "memory"); }
  const b8 H13_0 = asb(h13_0), H13_1 = asb(h13_1);
  SB();
  b8 f1287 = cat(l1287, h1287);
  MA(dX00, f1287, H13_0); SB();
  s4 l1291 = TR(pA0, 24576);
  SB();
  { asm volatile("s_waitcnt vmcnt(3)" : [pO]"+v"(pO) :  : "memory"); }
  repoll<384>(pO, rWS, v7, sFH, T_); SB();
  SB();
  MA(dX01, f1287, H13_1); SB();
  s4 h1291 = TR(pA0, 26624);
  SB();
  b8 f1288 = cat(l1288, h1288);
  MA(dX10, f1288, H13_0); SB();
  s4 l1292 = TR(pA1, 24576);
  SB();
  MA(dX11, f1288, H13_1); SB();
  s4 h1292 = TR(pA1, 26624);
  SB();
  b8 f1289 = cat(l1289, h1289);
  MA(dX20, f1289, H13_0); SB();
  s4 l1293 = TR(pA2, 24576);
  SB();
  const unsigned sO = 14680064u + ((Rb * 16 + 4 * gi + 2 * cg) * 4 + r) * 2048, sO2 = sO + 8192, sOL = cg ? FAR : 16777216u + ((Rb * 4 + gi) * 4 + r) * 1024;
  u4 oa, ob, oc, od; u2 oe, of; unsigned pT = v16 >> 1; OPQ(pT);
  { asm volatile("s_nop 2\n\tbuffer_load_dwordx4 %[oa], %[v16], %[rWS], %[sO] offen sc1\n\tbuffer_load_dwordx4 %[ob], %[v16], %[rWS], %[sO] offen offset:1024 sc1\n\tbuffer_load_dwordx4 %[oc], %[v16], %[rWS], %[sO2] offen sc1\n\tbuffer_load_dwordx4 %[od], %[v16], %[rWS], %[sO2] offen offset:1024 sc1\n\tbuffer_load_dwordx2 %[oe], %[pT], %[rWS], %[sOL] offen offset:0 sc1\n\tbuffer_load_dwordx2 %[of], %[pT], %[rWS], %[sOL] offen offset:512 sc1" : [oa]"=&v"(oa), [ob]"=&v"(ob), [oc]"=&v"(oc), [od]"=&v"(od), [oe]"=&v"(oe), [of]"=&v"(of) : [v16]"v"(v16), [rWS]"s"(rWS), [sO]"s"(sO), [sO2]"s"(sO2), [pT]"v"(pT), [sOL]"s"(sOL) : "memory"); }
  SB();
  MA(dX21, f1289, H13_1); SB();
  s4 h1293 = TR(pA2, 26624);
  SB();
  b8 f1290 = cat(l1290, h1290);
  MA(dX30, f1290, H13_0); SB();
  s4 l1294 = TR(pA3, 24576);
  SB();
  MA(dX31, f1290, H13_1); SB();
  s4 h1294 = TR(pA3, 26624);
  SB();
  const b8 H14_0 = asb(hk30), H14_1 = asb(hk31);
  SB();
  b8 f1291 = cat(l1291, h1291);
  MA(dX00, f1291, H14_0); SB();
  s4 l1295 = TR(pA0, 32768);
  SB();
  MA(dX01, f1291, H14_1); SB();
  s4 h1295 = TR(pA0, 34816);
  SB();
  b8 f1292 = cat(l1292, h1292);
  MA(dX10, f1292, H14_0); SB();
  s4 l1296 = TR(pA1, 32768);
  SB();
  MA(dX11, f1292, H14_1); SB();
  s4 h1296 = TR(pA1, 34816);
  SB();
  b8 f1293 = cat(l1293, h1293);
  MA(dX20, f1293, H14_0); SB();
  s4 l1297 = TR(pA2, 32768);
  SB();
  MA(dX21, f1293, H14_1); SB();
  s4 h1297 = TR(pA2, 34816);
  SB();
  b8 f1294 = cat(l1294, h1294);
  MA(dX30, f1294, H14_0); SB();
  s4 l1298 = TR(pA3, 32768);
  SB();
  MA(dX31, f1294, H14_1); SB();
  s4 h1298 = TR(pA3, 34816);
  SB();
  { asm volatile("s_waitcnt vmcnt(24)" : [h15_0]"+v"(h15_0), [h15_1]"+v"(h15_1) :  : "memory"); }
  const b8 H15_0 = asb(h15_0), H15_1 = asb(h15_1);
  SB();
  b8 f1295 = cat(l1295, h1295);
  MA(dX00, f1295, H15_0); SB();
  s4 l1299 = TR(pA0, 16384);
  SB();
  unsigned pB0 = (v16 & 0x3c0) | (lb + cg * 2048 + 98304u); OPQ(pB0);
  SB();
  pB0 |= pT & 0x18; OPQ(pB0); SB(); pT += lb + cg * 1024 + 57344u; OPQ(pT);
  SB();
  MA(dX01, f1295, H15_1); SB();
  s4 h1299 = TR(pA0, 18432);
  SB();
  b8 f1296 = cat(l1296, h1296);
  MA(dX10, f1296, H15_0); SB();
  s4 l1300 = TR(pA1, 16384);
  SB();
  pB0 |= (v16 >> 3) & 32; unsigned pB1 = pB0 ^ 32; OPQ(pB0); OPQ(pB1);
  SB();
  MA(dX11, f1296, H15_1); SB();
  s4 h1300 = TR(pA1, 18432);
  SB();
  b8 f1297 = cat(l1297, h1297);
  MA(dX20, f1297, H15_0); SB();
  s4 l1301 = TR(pA2, 16384);
  SB();
  MA(dX21, f1297, H15_1); SB();
  s4 h1301 = TR(pA2, 18432);
  SB();
  b8 f1298 = cat(l1298, h1298);
  MA(dX30, f1298, H15_0); SB();
  s4 l1302 = TR(pA3, 16384);
  SB();
  { unsigned t_; asm volatile("s_add_u32 m0, %[wb], 155648\n\ts_mov_b32 %[t], 40960\n\tbuffer_load_dwordx4 %[vL], %[rPK], %[t] offen sc1 lds" : [t]"=&s"(t_) : [wb]"s"(wb), [vL]"v"(vL), [rPK]"s"(rPK) : "memory", "scc"); }
  SB();
  MA(dX31, f1298, H15_1); SB();
  s4 h1302 = TR(pA3, 18432);
  SB();
  { asm volatile("s_waitcnt vmcnt(23)" : [h16_0]"+v"(h16_0), [h16_1]"+v"(h16_1) :  : "memory"); }
  const b8 H16_0 = asb(h16_0), H16_1 = asb(h16_1);
  SB();
  b8 f1299 = cat(l1299, h1299);
  MA(dX00, f1299, H16_0); SB();
  pA0 += 24576u; OPQ(pA0);
  s4 l1303 = TR(pA0, 40960);
  SB();
  MA(dX01, f1299, H16_1); SB();
  s4 h1303 = TR(pA0, 43008);
  SB();
  b8 f1300 = cat(l1300, h1300);
  MA(dX10, f1300, H16_0); SB();
  pA1 += 24576u; OPQ(pA1);
  s4 l1304 = TR(pA1, 40960);
  SB();
  MA(dX11, f1300, H16_1); SB();
  s4 h1304 = TR(pA1, 43008);
  SB();
  { unsigned t_; asm volatile("s_add_u32 m0, %[wb], 139264\n\ts_mov_b32 %[t], 49152\n\tbuffer_load_dwordx4 %[vL], %[rPK], %[t] offen sc1 lds" : [t]"=&s"(t_) : [wb]"s"(wb), [vL]"v"(vL), [rPK]"s"(rPK) : "memory", "scc"); }
  SB();
  b8 f1301 = cat(l1301, h1301);
  MA(dX20, f1301, H16_0); SB();
  pA2 += 24576u; OPQ(pA2);
  s4 l1305 = TR(pA2, 40960);
  SB();
  MA(dX21, f1301, H16_1); SB();
  s4 h1305 = TR(pA2, 43008);
  SB();
  b8 f1302 = cat(l1302, h1302);
  MA(dX30, f1302, H16_0); SB();
  pA3 += 24576u; OPQ(pA3);
  s4 l1306 = TR(pA3, 40960);
  SB();
  MA(dX31, f1302, H16_1); SB();
  s4 h1306 = TR(pA3, 43008);
  SB();
  { asm volatile("s_waitcnt vmcnt(21)" : [h17_0]"+v"(h17_0), [h17_1]"+v"(h17_1) :  : "memory"); }
  const b8 H17_0 = asb(h17_0), H17_1 = asb(h17_1);
  SB();
  b8 f1303 = cat(l1303, h1303);
  MA(dX00, f1303, H17_0); SB();
  s4 l1307 = TR(pA0, 16384);
  SB();
  { unsigned t_; asm volatile("s_add_u32 m0, %[wb], 147456\n\ts_mov_b32 %[t], 57344\n\tbuffer_load_dwordx4 %[vL], %[rPK], %[t] offen sc1 lds" : [t]"=&s"(t_) : [wb]"s"(wb), [vL]"v"(vL), [rPK]"s"(rPK) : "memory", "scc"); }
  SB();
  MA(dX01, f1303, H17_1); SB();
  s4 h1307 = TR(pA0, 18432);
  SB();
  { asm volatile("s_waitcnt vmcnt(3)" : [oa]"+v"(oa), [ob]"+v"(ob), [oc]"+v"(oc), [od]"+v"(od), [oe]"+v"(oe), [of]"+v"(of) :  : "memory"); }
  u4 H18_0u = {oa[0], oa[1], ob[0], ob[1]}, H18_1u = {oa[2], oa[3], ob[2], ob[3]}; OPQ(H18_0u); OPQ(H18_1u);
  u4 H19_0u = {oc[0], oc[1], od[0], od[1]}, H19_1u = {oc[2], oc[3], od[2], od[3]}; OPQ(H19_0u); OPQ(H19_1u);
  const b8 H18_0 = asb(H18_0u), H18_1 = asb(H18_1u), H19_0 = asb(H19_0u), H19_1 = asb(H19_1u); u2 HL0 = {oe[0], of[0]}, HL1 = {oe[1], of[1]}; OPQ(HL0); OPQ(HL1);
  SB();
  b8 f1304 = cat(l1304, h1304);
  MA(dX10, f1304, H17_0); SB();
  s4 l1308 = TR(pA1, 16384);
  SB();
  MA(dX11, f1304, H17_1); SB();
  s4 h1308 = TR(pA1, 18432);
  SB();
  SB();
  b8 f1305 = cat(l1305, h1305);
  MA(dX20, f1305, H17_0); SB();
  s4 l1309 = TR(pA2, 16384);
  SB();
  MA(dX21, f1305, H17_1); SB();
  s4 h1309 = TR(pA2, 18432);
  SB();
  { unsigned t_; asm volatile("s_add_u32 m0, %[wb], 131072\n\ts_mov_b32 %[t], 65536\n\tbuffer_load_dwordx4 %[vL], %[rPK], %[t] offen sc1 lds" : [t]"=&s"(t_) : [wb]"s"(wb), [vL]"v"(vL), [rPK]"s"(rPK) : "memory", "scc"); }
  SB();
  SB();
  b8 f1306 = cat(l1306, h1306);
  MA(dX30, f1306, H17_0); SB();
  s4 l1310 = TR(pA3, 16384);
  SB();
  MA(dX31, f1306, H17_1); SB();
  s4 h1310 = TR(pA3, 18432);
  SB();
  SB();
  b8 f1307 = cat(l1307, h1307);
  MA(dX00, f1307, H18_0); SB();
  s4 l1311 = TR(pA0, 49152);
  SB();
  MA(dX01, f1307, H18_1); SB();
  s4 h1311 = TR(pA0, 51200);
  SB();
  SB();
  b8 f1308 = cat(l1308, h1308);
  MA(dX10, f1308, H18_0); SB();
  s4 l1312 = TR(pA1, 49152);
  SB();
  { unsigned t_; asm volatile("s_add_u32 m0, %[wb], 122880\n\ts_mov_b32 %[t], 73728\n\tbuffer_load_dwordx4 %[vL], %[rPK], %[t] offen sc1 lds" : [t]"=&s"(t_) : [wb]"s"(wb), [vL]"v"(vL), [rPK]"s"(rPK) : "memory", "scc"); }
  SB();
  MA(dX11, f1308, H18_1); SB();
  s4 h1312 = TR(pA1, 51200);
  SB();
  b8 f1309 = cat(l1309, h1309);
  MA(dX20, f1309, H18_0); SB();
  s4 l1313 = TR(pA2, 49152);
  SB();
  MA(dX21, f1309, H18_1); SB();
  s4 h1313 = TR(pA2, 51200);
  SB();
  b8 f1310 = cat(l1310, h1310);
  MA(dX30, f1310, H18_0); SB();
  s4 l1314 = TR(pA3, 49152);
  SB();
  MA(dX31, f1310, H18_1); SB();
  s4 h1314 = TR(pA3, 51200);
  SB();
  { unsigned t_; asm volatile("s_add_u32 m0, %[wb], 49152\n\ts_mov_b32 %[t], 106496\n\tbuffer_load_dwordx4 %[vL], %[rPK], %[t] offen sc1 lds" : [t]"=&s"(t_) : [wb]"s"(wb), [vL]"v"(vL), [rPK]"s"(rPK) : "memory", "scc"); }
  SB();
  b8 f1311 = cat(l1311, h1311);
  MA(dX00, f1311, H19_0); SB();
  s4 l1315 = TR(pA0, 57344);
  SB();
  MA(dX01, f1311, H19_1); SB();
  b8 f1312 = cat(l1312, h1312);
  MA(dX10, f1312, H19_0); SB();
  s4 l1316 = TR(pA1, 57344);
  SB();
  MA(dX11, f1312, H19_1); SB();
  b8 f1313 = cat(l1313, h1313);
  MA(dX20, f1313, H19_0); SB();
  s4 l1317 = TR(pA2, 57344);
  SB();
  MA(dX21, f1313, H19_1); SB();
  b8 f1314 = cat(l1314, h1314);
  MA(dX30, f1314, H19_0); SB();
  s4 l1318 = TR(pA3, 57344);
  SB();
  MA(dX31, f1314, H19_1); SB();
  { asm volatile("s_waitcnt vmcnt(13)\n\ts_barrier" :  :  : "memory"); }
  SB();
  MH(dX00, l1315, HL0); SB();
  s4 l1321 = TR(pT, 57344);
  SB();
  { unsigned t_; asm volatile("s_add_u32 m0, %[wb], 40960\n\ts_mov_b32 %[t], 81920\n\tbuffer_load_dwordx4 %[vL], %[rPK], %[t] offen sc1 lds" : [t]"=&s"(t_) : [wb]"s"(wb), [vL]"v"(vL), [rPK]"s"(rPK) : "memory", "scc"); }
  SB();
  MH(dX01, l1315, HL1); SB();
  s4 h1321 = TR(pT, 57856);
  SB();
  MH(dX10, l1316, HL0); SB();
  s4 l1324 = TR(pT, 59392);
  SB();
  MH(dX11, l1316, HL1); SB();
  s4 h1324 = TR(pT, 59904);
  SB();
  MH(dX20, l1317, HL0); SB();
  s4 l1327 = TR(pT, 61440);
  SB();
  MH(dX21, l1317, HL1); SB();
  s4 h1327 = TR(pT, 61952);
  SB();
  { unsigned t_; asm volatile("s_add_u32 m0, %[wb], 32768\n\ts_mov_b32 %[t], 90112\n\tbuffer_load_dwordx4 %[vL], %[rPK], %[t] offen sc1 lds" : [t]"=&s"(t_) : [wb]"s"(wb), [vL]"v"(vL), [rPK]"s"(rPK) : "memory", "scc"); }
  SB();
  MH(dX30, l1318, HL0); SB();
  s4 l1330 = TR(pT, 63488);
  SB();
  MH(dX31, l1318, HL1); SB();
  s4 h1330 = TR(pT, 64000);
  SB();
  SB();
  b8 f1321 = cat(l1321, h1321);
  MZ(dT0, f1321, H0_0); SB();
  s4 l1333 = TR(pT, 0);
  SB();
  MZ(dT1, f1321, H0_1); SB();
  s4 h1333 = TR(pT, 512);
  SB();
  b8 f1324 = cat(l1324, h1324);
  MA(dT0, f1324, H1_0); SB();
  s4 l1336 = TR(pT, 2048);
  SB();
  { unsigned t_; asm volatile("s_add_u32 m0, %[wb], 65536\n\ts_mov_b32 %[t], 98304\n\tbuffer_load_dwordx4 %[vL], %[rPK], %[t] offen sc1 lds" : [t]"=&s"(t_) : [wb]"s"(wb), [vL]"v"(vL), [rPK]"s"(rPK) : "memory", "scc"); }
  SB();
  MA(dT1, f1324, H1_1); SB();
  s4 h1336 = TR(pT, 2560);
  SB();
  b8 f1327 = cat(l1327, h1327);
  MA(dT0, f1327, H2_0); SB();
  s4 l1339 = TR(pT, 4096);
  SB();
  unsigned ex0 = lb + 0 + (w ^ 0) * 4096 + v16; OPQ(ex0);
  SB();
  LR(ex0, 0) = __builtin_bit_cast(u4, dX01);
  SB();
  LR(ex0, 1024) = __builtin_bit_cast(u4, dX11);
  SB();
  MA(dT1, f1327, H2_1); SB();
  s4 h1339 = TR(pT, 4608);
  SB();
  LR(ex0, 2048) = __builtin_bit_cast(u4, dX21);
  SB();
  b8 f1330 = cat(l1330, h1330);
  MA(dT0, f1330, H3_0); SB();
  s4 l1342 = TR(pT, 6144);
  SB();
  LR(ex0, 3072) = __builtin_bit_cast(u4, dX31);
  SB();
  SB();
  MA(dT1, f1330, H3_1); SB();
  s4 h1342 = TR(pT, 6656);
  SB();
  { unsigned t_; asm volatile("s_add_u32 m0, %[wb], 73728\n\ts_mov_b32 %[t], 114688\n\tbuffer_load_dwordx4 %[vL], %[rPK], %[t] offen sc1 lds" : [t]"=&s"(t_) : [wb]"s"(wb), [vL]"v"(vL), [rPK]"s"(rPK) : "memory", "scc"); }
  SB();
  b8 f1333 = cat(l1333, h1333);
  MA(dT0, f1333, H4_0); SB();
  s4 l1345 = TR(pT, 32768);
  SB();
  MA(dT1, f1333, H4_1); SB();
  s4 h1345 = TR(pT, 33280);
  SB();
  b8 f1336 = cat(l1336, h1336);
  MA(dT0, f1336, H5_0); SB();
  s4 l1348 = TR(pT, 34816);
  SB();
  MA(dT1, f1336, H5_1); SB();
  s4 h1348 = TR(pT, 35328);
  SB();
  b8 f1339 = cat(l1339, h1339);
  MA(dT0, f1339, H6_0); SB();
  s4 l1351 = TR(pT, 36864);
  SB();
  MA(dT1, f1339, H6_1); SB();
  s4 h1351 = TR(pT, 37376);
  SB();
  b8 f1342 = cat(l1342, h1342);
  MA(dT0, f1342, H7_0); SB();
  s4 l1354 = TR(pT, 38912);
  SB();
  MA(dT1, f1342, H7_1); SB();
  s4 h1354 = TR(pT, 39424);
  SB();
  b8 f1345 = cat(l1345, h1345);
  MA(dT0, f1345, H8_0); SB();
  s4 l1357 = TR(pT, 49152);
  SB();
  MA(dT1, f1345, H8_1); SB();
  s4 h1357 = TR(pT, 49664);
  SB();
  b8 f1348 = cat(l1348, h1348);
  MA(dT0, f1348, H9_0); SB();
  s4 l1360 = TR(pT, 51200);
  SB();
  MA(dT1, f1348, H9_1); SB();
  s4 h1360 = TR(pT, 51712);
  SB();
  b8 f1351 = cat(l1351, h1351);
  MA(dT0, f1351, H10_0); SB();
  s4 l1363 = TR(pT, 53248);
  SB();
  MA(dT1, f1351, H10_1); SB();
  s4 h1363 = TR(pT, 53760);
  SB();
  b8 f1354 = cat(l1354, h1354);
  MA(dT0, f1354, H11_0); SB();
  s4 l1366 = TR(pT, 55296);
  SB();
  MA(dT1, f1354, H11_1); SB();
  s4 h1366 = TR(pT, 55808);
  SB();
  { asm volatile("s_waitcnt vmcnt(5)\n\ts_barrier" :  :  : "memory"); }
  SB();
  b8 f1357 = cat(l1357, h1357);
  MA(dT0, f1357, H12_0); SB();
  s4 l1369 = TR(pT, 40960);
  SB();
  { unsigned t_; asm volatile("s_add_u32 m0, %[wb], 90112\n\ts_mov_b32 %[t], 122880\n\tbuffer_load_dwordx4 %[vL], %[rPK], %[t] offen sc1 lds" : [t]"=&s"(t_) : [wb]"s"(wb), [vL]"v"(vL), [rPK]"s"(rPK) : "memory", "scc"); }
  SB();
  MA(dT1, f1357, H12_1); SB();
  s4 h1369 = TR(pT, 41472);
  SB();
  unsigned ex1 = lb + 0 + (w ^ 4) * 4096 + v16; OPQ(ex1);
  SB();
  const f4 e3010_0 = __builtin_bit_cast(f4, LR(ex1, 0));
  SB();
  const f4 e3010_1 = __builtin_bit_cast(f4, LR(ex1, 1024));
  SB();
  b8 f1360 = cat(l1360, h1360);
  MA(dT0, f1360, H13_0); SB();
  s4 l1372 = TR(pT, 43008);
  SB();

  SB();
  MA(dT1, f1360, H13_1); SB();
  s4 h1372 = TR(pT, 43520);
  SB();

  SB();
  b8 f1363 = cat(l1363, h1363);
  MA(dT0, f1363, H14_0); SB();
  s4 l1375 = TR(pT, 45056);
  SB();
  { float t_ = dX00[0] + e3010_0[0]; OPQ(t_); SB(); dX00[0] = t_; } { float t_ = dX00[1] + e3010_0[1]; OPQ(t_); SB(); dX00[1] = t_; }
  SB();
  MA(dT1, f1363, H14_1); SB();
  s4 h1375 = TR(pT, 45568);
  SB();
  { unsigned t_; asm volatile("s_add_u32 m0, %[wb], 57344\n\ts_mov_b32 %[t], 131072\n\tbuffer_load_dwordx4 %[vL], %[rPK], %[t] offen sc1 lds" : [t]"=&s"(t_) : [wb]"s"(wb), [vL]"v"(vL), [rPK]"s"(rPK) : "memory", "scc"); }
  SB();
  b8 f1366 = cat(l1366, h1366);
  MA(dT0, f1366, H15_0); SB();
  s4 l1378 = TR(pT, 47104);
  SB();
  { float t_ = dX00[2] + e3010_0[2]; OPQ(t_); SB(); dX00[2] = t_; } { float t_ = dX00[3] + e3010_0[3]; OPQ(t_); SB(); dX00[3] = t_; }
  SB();
  { float t_ = dX10[0] + e3010_1[0]; OPQ(t_); SB(); dX10[0] = t_; } { float t_ = dX10[1] + e3010_1[1]; OPQ(t_); SB(); dX10[1] = t_; }
  SB();
  MA(dT1, f1366, H15_1); SB();
  s4 h1378 = TR(pT, 47616);
  SB();
  { float t_ = dX10[2] + e3010_1[2]; OPQ(t_); SB(); dX10[2] = t_; } { float t_ = dX10[3] + e3010_1[3]; OPQ(t_); SB(); dX10[3] = t_; }
  SB();
  const unsigned sPU = 17825792u + ((Rb * 4 + j) * 16 + gi) * 8192 + w * 1024;
  SB();
  b8 f1369 = cat(l1369, h1369);
  MA(dT0, f1369, H16_0); SB();
  s4 l1381 = TR(pT, 28672);
  SB();
  st16((cat2(pk4(dX00), pk4(dX10))), rWS, v16, sPU + ((gi ^ 2) & 2) * 32768);
  SB();
  MA(dT1, f1369, H16_1); SB();
  b8 f1372 = cat(l1372, h1372);
  MA(dT0, f1372, H17_0); SB();
  s4 l1382 = TR(pB0, 57344);
  SB();
  { unsigned t_; asm volatile("s_add_u32 m0, %[wb], 114688\n\ts_mov_b32 %[t], 139264\n\tbuffer_load_dwordx4 %[vL], %[rPK], %[t] offen sc1 lds" : [t]"=&s"(t_) : [wb]"s"(wb), [vL]"v"(vL), [rPK]"s"(rPK) : "memory", "scc"); }
  SB();
  MA(dT1, f1372, H17_1); SB();
  s4 h1382 = TR(pB0, 58368);
  SB();
  const f4 e3011_0 = __builtin_bit_cast(f4, LR(ex1, 2048));
  SB();
  const f4 e3011_1 = __builtin_bit_cast(f4, LR(ex1, 3072));
  SB();
  b8 f1375 = cat(l1375, h1375);
  MA(dT0, f1375, H18_0); SB();
  s4 l1383 = TR(pB1, 57344);
  SB();

  SB();
  MA(dT1, f1375, H18_1); SB();
  s4 h1383 = TR(pB1, 58368);
  SB();

  SB();
  b8 f1378 = cat(l1378, h1378);
  MA(dT0, f1378, H19_0); SB();
  s4 l1385 = TR(pB0, 61440);
  SB();
  { float t_ = dX20[0] + e3011_0[0]; OPQ(t_); SB(); dX20[0] = t_; } { float t_ = dX20[1] + e3011_0[1]; OPQ(t_); SB(); dX20[1] = t_; }
  SB();
  MA(dT1, f1378, H19_1); SB();
  s4 h1385 = TR(pB0, 62464);
  SB();
  { float t_ = dX20[2] + e3011_0[2]; OPQ(t_); SB(); dX20[2] = t_; } { float t_ = dX20[3] + e3011_0[3]; OPQ(t_); SB(); dX20[3] = t_; }
  SB();
  MH(dT0, l1381, HL0); SB();
  s4 l1386 = TR(pB1, 61440);
  SB();
  { float t_ = dX30[0] + e3011_1[0]; OPQ(t_); SB(); dX30[0] = t_; } { float t_ = dX30[1] + e3011_1[1]; OPQ(t_); SB(); dX30[1] = t_; }
  SB();
  MH(dT1, l1381, HL1); SB();
  s4 h1386 = TR(pB1, 62464);
  SB();
  { float t_ = dX30[2] + e3011_1[2]; OPQ(t_); SB(); dX30[2] = t_; } { float t_ = dX30[3] + e3011_1[3]; OPQ(t_); SB(); dX30[3] = t_; }
  SB();
  b8 f1382 = cat(l1382, h1382);
  MZ(dA00, f1382, H0_0); SB();
  s4 l1388 = TR(pB0, 40960);
  SB();
  st16((cat2(pk4(dX20), pk4(dX30))), rWS, v16, sPU + (((gi ^ 2) & 2) + 1) * 32768);
  SB();
  MZ(dA01, f1382, H0_1); SB();
  s4 h1388 = TR(pB0, 41984);
  SB();
  b8 f1383 = cat(l1383, h1383);
  MZ(dA10, f1383, H0_0); SB();
  s4 l1389 = TR(pB1, 40960);
  SB();
  MZ(dA11, f1383, H0_1); SB();
  s4 h1389 = TR(pB1, 41984);
  SB();
  b8 f1385 = cat(l1385, h1385);
  MA(dA00, f1385, H1_0); SB();
  s4 l1391 = TR(pB0, 45056);
  SB();
  u4 tz;
  SB();
  tz[0] = ({ float a_ = dT0[0] * G0; OPQ(a_); SB(); float b_ = dT0[1] * G0; pk(a_, b_); });
  SB();
  tz[1] = ({ float a_ = dT0[2] * G0; OPQ(a_); SB(); float b_ = dT0[3] * G0; pk(a_, b_); });
  SB();
  MA(dA01, f1385, H1_1); SB();
  s4 h1391 = TR(pB0, 46080);
  SB();
  tz[2] = ({ float a_ = dT1[0] * G1; OPQ(a_); SB(); float b_ = dT1[1] * G1; pk(a_, b_); });
  SB();
  b8 f1386 = cat(l1386, h1386);
  MA(dA10, f1386, H1_0); SB();
  s4 l1392 = TR(pB1, 45056);
  SB();
  MA(dA11, f1386, H1_1); SB();
  s4 h1392 = TR(pB1, 46080);
  SB();
  tz[3] = ({ float a_ = dT1[2] * G1; OPQ(a_); SB(); float b_ = dT1[3] * G1; pk(a_, b_); });
  SB();
  b8 f1388 = cat(l1388, h1388);
  MA(dA00, f1388, H2_0); SB();
  s4 l1394 = TR(pB0, 49152);
  SB();
  st16(tz, rWS, v16, 26214400u + ((((Rb * 4 + j) * 4 + r) * 2 + cg) * 4 + gi) * 1024);
  SB();
  MA(dA01, f1388, H2_1); SB();
  s4 h1394 = TR(pB0, 50176);
  SB();
  b8 f1389 = cat(l1389, h1389);
  MA(dA10, f1389, H2_0); SB();
  s4 l1395 = TR(pB1, 49152);
  SB();
  MA(dA11, f1389, H2_1); SB();
  s4 h1395 = TR(pB1, 50176);
  SB();
  b8 f1391 = cat(l1391, h1391);
  MA(dA00, f1391, H3_0); SB();
  s4 l1397 = TR(pB0, 53248);
  SB();
  MA(dA01, f1391, H3_1); SB();
  s4 h1397 = TR(pB0, 54272);
  SB();
  b8 f1392 = cat(l1392, h1392);
  MA(dA10, f1392, H3_0); SB();
  s4 l1398 = TR(pB1, 53248);
  SB();
  MA(dA11, f1392, H3_1); SB();
  s4 h1398 = TR(pB1, 54272);
  SB();
  b8 f1394 = cat(l1394, h1394);
  MA(dA00, f1394, H4_0); SB();
  s4 l1400 = TR(pB0, 32768);
  SB();
  MA(dA01, f1394, H4_1); SB();
  s4 h1400 = TR(pB0, 33792);
  SB();
  b8 f1395 = cat(l1395, h1395);
  MA(dA10, f1395, H4_0); SB();
  s4 l1401 = TR(pB1, 32768);
  SB();
  MA(dA11, f1395, H4_1); SB();
  s4 h1401 = TR(pB1, 33792);
  SB();
  b8 f1397 = cat(l1397, h1397);
  MA(dA00, f1397, H5_0); SB();
  s4 l1403 = TR(pB0, 36864);
  SB();
  MA(dA01, f1397, H5_1); SB();
  s4 h1403 = TR(pB0, 37888);
  SB();
  b8 f1398 = cat(l1398, h1398);
  MA(dA10, f1398, H5_0); SB();
  s4 l1404 = TR(pB1, 36864);
  SB();
  MA(dA11, f1398, H5_1); SB();
  s4 h1404 = TR(pB1, 37888);
  SB();
  b8 f1400 = cat(l1400, h1400);
  MA(dA00, f1400, H6_0); SB();
  s4 l1406 = TR(pB0, 24576);
  SB();
  MA(dA01, f1400, H6_1); SB();
  s4 h1406 = TR(pB0, 25600);
  SB();
  b8 f1401 = cat(l1401, h1401);
  MA(dA10, f1401, H6_0); SB();
  s4 l1407 = TR(pB1, 24576);
  SB();
  MA(dA11, f1401, H6_1); SB();
  s4 h1407 = TR(pB1, 25600);
  SB();
  b8 f1403 = cat(l1403, h1403);
  MA(dA00, f1403, H7_0); SB();
  s4 l1409 = TR(pB0, 28672);
  SB();
  MA(dA01, f1403, H7_1); SB();
  s4 h1409 = TR(pB0, 29696);
  SB();
  b8 f1404 = cat(l1404, h1404);
  MA(dA10, f1404, H7_0); SB();
  s4 l1410 = TR(pB1, 28672);
  SB();
  MA(dA11, f1404, H7_1); SB();
  s4 h1410 = TR(pB1, 29696);
  SB();
  { asm volatile("s_waitcnt vmcnt(6)\n\ts_barrier" :  :  : "memory"); }
  SB();
  b8 f1406 = cat(l1406, h1406);
  MA(dA00, f1406, H8_0); SB();
  pB0 += 4294901760u; OPQ(pB0);
  s4 l1412 = TR(pB0, 8192);
  SB();
  { unsigned t_; asm volatile("s_add_u32 m0, %[wb], 131072\n\ts_mov_b32 %[t], 147456\n\tbuffer_load_dwordx4 %[vL], %[rPK], %[t] offen sc1 lds" : [t]"=&s"(t_) : [wb]"s"(wb), [vL]"v"(vL), [rPK]"s"(rPK) : "memory", "scc"); }
  SB();
  MA(dA01, f1406, H8_1); SB();
  s4 h1412 = TR(pB0, 9216);
  SB();
  b8 f1407 = cat(l1407, h1407);
  MA(dA10, f1407, H8_0); SB();
  pB1 += 4294901760u; OPQ(pB1);
  s4 l1413 = TR(pB1, 8192);
  SB();
  MA(dA11, f1407, H8_1); SB();
  s4 h1413 = TR(pB1, 9216);
  SB();
  b8 f1409 = cat(l1409, h1409);
  MA(dA00, f1409, H9_0); SB();
  s4 l1415 = TR(pB0, 12288);
  SB();
  MA(dA01, f1409, H9_1); SB();
  s4 h1415 = TR(pB0, 13312);
  SB();
  { unsigned t_; asm volatile("s_add_u32 m0, %[wb], 106496\n\ts_mov_b32 %[t], 155648\n\tbuffer_load_dwordx4 %[vL], %[rPK], %[t] offen sc1 lds" : [t]"=&s"(t_) : [wb]"s"(wb), [vL]"v"(vL), [rPK]"s"(rPK) : "memory", "scc"); }
  SB();
  b8 f1410 = cat(l1410, h1410);
  MA(dA10, f1410, H9_0); SB();
  s4 l1416 = TR(pB1, 12288);
  SB();
  MA(dA11, f1410, H9_1); SB();
  s4 h1416 = TR(pB1, 13312);
  SB();
  const unsigned sGP = sFP + (gi * 2 + cg) * 16;
  { asm volatile("s_waitcnt vmcnt(2)" :  :  : "memory"); }
  st8((u2){tlo, thi}, rWS, vF + 0, sGP); SB();
  SB();
  b8 f1412 = cat(l1412, h1412);
  MA(dA00, f1412, H10_0); SB();
  s4 l1418 = TR(pB0, 0);
  SB();
  MA(dA01, f1412, H10_1); SB();
  s4 h1418 = TR(pB0, 1024);
  SB();
  b8 f1413 = cat(l1413, h1413);
  MA(dA10, f1413, H10_0); SB();
  s4 l1419 = TR(pB1, 0);
  SB();
  { unsigned t_; asm volatile("s_add_u32 m0, %[wb], 155648\n\ts_mov_b32 %[t], 163840\n\tbuffer_load_dwordx4 %[vL], %[rPK], %[t] offen sc1 lds" : [t]"=&s"(t_) : [wb]"s"(wb), [vL]"v"(vL), [rPK]"s"(rPK) : "memory", "scc"); }
  SB();
  MA(dA11, f1413, H10_1); SB();
  s4 h1419 = TR(pB1, 1024);
  SB();
  b8 f1415 = cat(l1415, h1415);
  MA(dA00, f1415, H11_0); SB();
  s4 l1421 = TR(pB0, 4096);
  SB();
  MA(dA01, f1415, H11_1); SB();
  s4 h1421 = TR(pB0, 5120);
  SB();
  b8 f1416 = cat(l1416, h1416);
  MA(dA10, f1416, H11_0); SB();
  s4 l1422 = TR(pB1, 4096);
  SB();
  MA(dA11, f1416, H11_1); SB();
  s4 h1422 = TR(pB1, 5120);
  SB();
  { unsigned t_; asm volatile("s_add_u32 m0, %[wb], 139264\n\ts_mov_b32 %[t], 172032\n\tbuffer_load_dwordx4 %[vL], %[rPK], %[t] offen sc1 lds" : [t]"=&s"(t_) : [wb]"s"(wb), [vL]"v"(vL), [rPK]"s"(rPK) : "memory", "scc"); }
  SB();
  b8 f1418 = cat(l1418, h1418);
  MA(dA00, f1418, H12_0); SB();
  s4 l1424 = TR(pB0, 32768);
  SB();
  MA(dA01, f1418, H12_1); SB();
  s4 h1424 = TR(pB0, 33792);
  SB();
  b8 f1419 = cat(l1419, h1419);
  MA(dA10, f1419, H12_0); SB();
  s4 l1425 = TR(pB1, 32768);
  SB();
  MA(dA11, f1419, H12_1); SB();
  s4 h1425 = TR(pB1, 33792);
  SB();
  b8 f1421 = cat(l1421, h1421);
  MA(dA00, f1421, H13_0); SB();
  s4 l1427 = TR(pB0, 36864);
  SB();
  { unsigned t_; asm volatile("s_add_u32 m0, %[wb], 147456\n\ts_mov_b32 %[t], 180224\n\tbuffer_load_dwordx4 %[vL], %[rPK], %[t] offen sc1 lds" : [t]"=&s"(t_) : [wb]"s"(wb), [vL]"v"(vL), [rPK]"s"(rPK) : "memory", "scc"); }
  SB();
  MA(dA01, f1421, H13_1); SB();
  s4 h1427 = TR(pB0, 37888);
  SB();
  b8 f1422 = cat(l1422, h1422);
  MA(dA10, f1422, H13_0); SB();
  s4 l1428 = TR(pB1, 36864);
  SB();
  MA(dA11, f1422, H13_1); SB();
  s4 h1428 = TR(pB1, 37888);
  SB();
  b8 f1424 = cat(l1424, h1424);
  MA(dA00, f1424, H14_0); SB();
  s4 l1430 = TR(pB0, 16384);
  SB();
  MA(dA01, f1424, H14_1); SB();
  s4 h1430 = TR(pB0, 17408);
  SB();
  { unsigned t_; asm volatile("s_add_u32 m0, %[wb], 98304\n\ts_mov_b32 %[t], 188416\n\tbuffer_load_dwordx4 %[vL], %[rPK], %[t] offen sc1 lds" : [t]"=&s"(t_) : [wb]"s"(wb), [vL]"v"(vL), [rPK]"s"(rPK) : "memory", "scc"); }
  SB();
  b8 f1425 = cat(l1425, h1425);
  MA(dA10, f1425, H14_0); SB();
  s4 l1431 = TR(pB1, 16384);
  SB();
  MA(dA11, f1425, H14_1); SB();
  s4 h1431 = TR(pB1, 17408);
  SB();
  b8 f1427 = cat(l1427, h1427);
  MA(dA00, f1427, H15_0); SB();
  s4 l1433 = TR(pB0, 20480);
  SB();
  MA(dA01, f1427, H15_1); SB();
  s4 h1433 = TR(pB0, 21504);
  SB();
  b8 f1428 = cat(l1428, h1428);
  MA(dA10, f1428, H15_0); SB();
  s4 l1434 = TR(pB1, 20480);
  SB();
  MA(dA11, f1428, H15_1); SB();
  s4 h1434 = TR(pB1, 21504);
  SB();
  b8 f1430 = cat(l1430, h1430);
  MA(dA00, f1430, H16_0); SB();
  s4 l1436 = TR(pB0, 40960);
  SB();
  MA(dA01, f1430, H16_1); SB();
  s4 h1436 = TR(pB0, 41984);
  SB();
  b8 f1431 = cat(l1431, h1431);
  MA(dA10, f1431, H16_0); SB();
  s4 l1437 = TR(pB1, 40960);
  SB();
  MA(dA11, f1431, H16_1); SB();
  s4 h1437 = TR(pB1, 41984);
  SB();
  b8 f1433 = cat(l1433, h1433);
  MA(dA00, f1433, H17_0); SB();
  s4 l1439 = TR(pB0, 45056);
  SB();
  MA(dA01, f1433, H17_1); SB();
  s4 h1439 = TR(pB0, 46080);
  SB();
  b8 f1434 = cat(l1434, h1434);
  MA(dA10, f1434, H17_0); SB();
  s4 l1440 = TR(pB1, 45056);
  SB();
  MA(dA11, f1434, H17_1); SB();
  s4 h1440 = TR(pB1, 46080);
  SB();
  SB();
  b8 f1436 = cat(l1436, h1436);
  MA(dA00, f1436, H18_0); SB();
  s4 l1442 = TR(pB0, 51200);
  SB();
  MA(dA01, f1436, H18_1); SB();
  SB();
  b8 f1437 = cat(l1437, h1437);
  MA(dA10, f1437, H18_0); SB();
  s4 l1443 = TR(pB1, 51200);
  SB();
  MA(dA11, f1437, H18_1); SB();
  SB();
  { asm volatile("s_waitcnt vmcnt(5)\n\ts_barrier" :  :  : "memory"); }
  SB();
  b8 f1439 = cat(l1439, h1439);
  MA(dA00, f1439, H19_0); SB();
  s4 l1445 = TR(pB0, 57344);
  SB();
  { unsigned t_; asm volatile("s_add_u32 m0, %[wb], 32768\n\ts_mov_b32 %[t], 196608\n\tbuffer_load_dwordx4 %[vL], %[rPK], %[t] offen sc1 lds" : [t]"=&s"(t_) : [wb]"s"(wb), [vL]"v"(vL), [rPK]"s"(rPK) : "memory", "scc"); }
  SB();
  MA(dA01, f1439, H19_1); SB();
  s4 h1445 = TR(pB0, 58368);
  SB();
  b8 f1440 = cat(l1440, h1440);
  MA(dA10, f1440, H19_0); SB();
  s4 l1446 = TR(pB1, 57344);
  SB();
  MA(dA11, f1440, H19_1); SB();
  s4 h1446 = TR(pB1, 58368);
  SB();
  MH(dA00, l1442, HL0); SB();
  s4 l1447 = TR(pB0, 61440);
  SB();
  MH(dA01, l1442, HL1); SB();
  s4 h1447 = TR(pB0, 62464);
  SB();
  MH(dA10, l1443, HL0); SB();
  s4 l1448 = TR(pB1, 61440);
  SB();
  MH(dA11, l1443, HL1); SB();
  s4 h1448 = TR(pB1, 62464);
  SB();
  SB();
  b8 f1445 = cat(l1445, h1445);
  MZ(dB00, f1445, H0_0); SB();
  s4 l1449 = TR(pB0, 24576);
  SB();
  MZ(dB01, f1445, H0_1); SB();
  s4 h1449 = TR(pB0, 25600);
  SB();
  b8 f1446 = cat(l1446, h1446);
  MZ(dB10, f1446, H0_0); SB();
  s4 l1450 = TR(pB1, 24576);
  SB();
  MZ(dB11, f1446, H0_1); SB();
  s4 h1450 = TR(pB1, 25600);
  SB();
  b8 f1447 = cat(l1447, h1447);
  MA(dB00, f1447, H1_0); SB();
  s4 l1451 = TR(pB0, 28672);
  SB();
  st16((cat2(pk4(dA00), pk4(dA10))), rWS, v16, sPV + w * 2048);
  SB();
  st16((cat2(pk4(dA01), pk4(dA11))), rWS, v16, sPV + (w ^ 4) * 2048 + 1024);
  SB();
  MA(dB01, f1447, H1_1); SB();
  s4 h1451 = TR(pB0, 29696);
  SB();
  b8 f1448 = cat(l1448, h1448);
  MA(dB10, f1448, H1_0); SB();
  s4 l1452 = TR(pB1, 28672);
  SB();
  MA(dB11, f1448, H1_1); SB();
  s4 h1452 = TR(pB1, 29696);
  SB();
  b8 f1449 = cat(l1449, h1449);
  MA(dB00, f1449, H2_0); SB();
  pB0 += 65536u; OPQ(pB0);
  s4 l1453 = TR(pB0, 16384);
  SB();
  MA(dB01, f1449, H2_1); SB();
  s4 h1453 = TR(pB0, 17408);
  SB();
  b8 f1450 = cat(l1450, h1450);
  MA(dB10, f1450, H2_0); SB();
  pB1 += 65536u; OPQ(pB1);
  s4 l1454 = TR(pB1, 16384);
  SB();
  MA(dB11, f1450, H2_1); SB();
  s4 h1454 = TR(pB1, 17408);
  SB();
  b8 f1451 = cat(l1451, h1451);
  MA(dB00, f1451, H3_0); SB();
  s4 l1455 = TR(pB0, 20480);
  SB();
  MA(dB01, f1451, H3_1); SB();
  s4 h1455 = TR(pB0, 21504);
  SB();
  b8 f1452 = cat(l1452, h1452);
  MA(dB10, f1452, H3_0); SB();
  s4 l1456 = TR(pB1, 20480);
  SB();
  MA(dB11, f1452, H3_1); SB();
  s4 h1456 = TR(pB1, 21504);
  SB();
  b8 f1453 = cat(l1453, h1453);
  MA(dB00, f1453, H4_0); SB();
  s4 l1457 = TR(pB0, 32768);
  SB();
  unsigned va = v16 & 0x3c0, vT = v16 + 3 * va; OPQ(va); OPQ(vT);
  SB();
  u2 ix = __builtin_bit_cast(u2, 0x3f80ull << (v16 & 63)); u4 i2 = cat2(ix, ix); OPQ(i2);
  SB();
  MA(dB01, f1453, H4_1); SB();
  s4 h1457 = TR(pB0, 33792);
  SB();
  b8 f1454 = cat(l1454, h1454);
  MA(dB10, f1454, H4_0); SB();
  s4 l1458 = TR(pB1, 32768);
  SB();
  u2 iy = LN(0xf0000f0000f0000full) ? (u2){i2[0], i2[1]} : (u2){0, 0}; OPQ(iy);
  SB();
  MA(dB11, f1454, H4_1); SB();
  s4 h1458 = TR(pB1, 33792);
  SB();
  b8 f1455 = cat(l1455, h1455);
  MA(dB00, f1455, H5_0); SB();
  s4 l1459 = TR(pB0, 36864);
  SB();
  u6 iz = {0, 0, iy[0], iy[1], 0, 0}; OPQ(iz); SB(); const u4 i0 = __builtin_shufflevector(iz, iz, 2, 3, 4, 5), i1 = __builtin_shufflevector(iz, iz, 0, 1, 2, 3);
  SB();
  MA(dB01, f1455, H5_1); SB();
  s4 h1459 = TR(pB0, 37888);
  SB();
  b8 f1456 = cat(l1456, h1456);
  MA(dB10, f1456, H5_0); SB();
  s4 l1460 = TR(pB1, 36864);
  SB();
  MA(dB11, f1456, H5_1); SB();
  s4 h1460 = TR(pB1, 37888);
  SB();
  b8 f1457 = cat(l1457, h1457);
  MA(dB00, f1457, H6_0); SB();
  s4 l1461 = TR(pB0, 8192);
  SB();
  u2 pP0;
  { asm volatile("buffer_load_dwordx2 %[pP0], %[v7], %[rWS], %[sFP] offen offset:0 sc1" : [pP0]"=&v"(pP0) : [v7]"v"(v7), [rWS]"s"(rWS), [sFP]"s"(sFP) : "memory"); }
  SB();
  MA(dB01, f1457, H6_1); SB();
  s4 h1461 = TR(pB0, 9216);
  SB();
  const unsigned oB = lnY * 128 + (gi & 1) * 64, oR = (32 * r + 16 * cg) * 1152; const rsrc_t rO = mk(pout + Rb * 147456u + oR, 147456u - oR);
  unsigned vo = v16 + __umul24(va, 17); OPQ(vo); SB(); unsigned vt = v16 - va; OPQ(vt);
  SB();
  b8 f1458 = cat(l1458, h1458);
  MA(dB10, f1458, H6_0); SB();
  s4 l1462 = TR(pB1, 8192);
  SB();
  vt = LN(0xffffull) ? __umul24(vt, 72) + (va >> 3) : vF; OPQ(vt);
  SB();
  MA(dB11, f1458, H6_1); SB();
  s4 h1462 = TR(pB1, 9216);
  SB();
  b8 f1459 = cat(l1459, h1459);
  MA(dB00, f1459, H7_0); SB();
  s4 l1463 = TR(pB0, 12288);
  SB();
  MA(dB01, f1459, H7_1); SB();
  s4 h1463 = TR(pB0, 13312);
  SB();
  { asm volatile("s_waitcnt vmcnt(1)" :  :  : "memory"); }
  st8((u2){tlo, thi}, rWS, vF + 128, sGP); SB();
  SB();
  b8 f1460 = cat(l1460, h1460);
  MA(dB10, f1460, H7_0); SB();
  s4 l1464 = TR(pB1, 12288);
  SB();
  unsigned wa = ((v16 >> 5) & 0x18) | (lb + 0 + w * 4096); OPQ(wa);
  SB();
  MA(dB11, f1460, H7_1); SB();
  s4 h1464 = TR(pB1, 13312);
  SB();
  wa += (v16 & 0xf0) * 5; OPQ(wa);
  SB();
  { asm volatile("s_waitcnt vmcnt(4)\n\ts_barrier" :  :  : "memory"); }
  SB();
  b8 f1461 = cat(l1461, h1461);
  MA(dB00, f1461, H8_0); SB();
  s4 l1465 = TR(pB0, 57344);
  SB();
  MA(dB01, f1461, H8_1); SB();
  s4 h1465 = TR(pB0, 58368);
  SB();
  unsigned ra = ex0 + (va >> 2); OPQ(ra);
  SB();
  b8 f1462 = cat(l1462, h1462);
  MA(dB10, f1462, H8_0); SB();
  s4 l1466 = TR(pB1, 57344);
  SB();
  MA(dB11, f1462, H8_1); SB();
  s4 h1466 = TR(pB1, 58368);
  SB();
  b8 f1463 = cat(l1463, h1463);
  MA(dB00, f1463, H9_0); SB();
  s4 l1467 = TR(pB0, 61440);
  SB();
  MA(dB01, f1463, H9_1); SB();
  s4 h1467 = TR(pB0, 62464);
  SB();
  b8 f1464 = cat(l1464, h1464);
  MA(dB10, f1464, H9_0); SB();
  s4 l1468 = TR(pB1, 61440);
  SB();
  MA(dB11, f1464, H9_1); SB();
  s4 h1468 = TR(pB1, 62464);
  SB();
  b8 f1465 = cat(l1465, h1465);
  MA(dB00, f1465, H10_0); SB();
  s4 l1469 = TR(pB0, 40960);
  SB();
  { asm volatile("s_waitcnt vmcnt(1)" : [pP0]"+v"(pP0) :  : "memory"); }
  repoll<0>(pP0, rWS, v7, sFP, T_); SB();
  SB();
  MA(dB01, f1465, H10_1); SB();
  s4 h1469 = TR(pB0, 41984);
  SB();
  const unsigned sPR = 17825792u + ((Rb * 4 + j) * 4 + gi) * 32768 + w * 1024, sP2 = sPR + (gi ^ 2) * 8192, sP3 = sPR + (gi ^ 3) * 8192;
  u4 q2, q3;
  { asm volatile("s_nop 2\n\tbuffer_load_dwordx4 %[q2], %[v16], %[rWS], %[sP2] offen sc1\n\tbuffer_load_dwordx4 %[q3], %[v16], %[rWS], %[sP3] offen sc1" : [q2]"=&v"(q2), [q3]"=&v"(q3) : [v16]"v"(v16), [rWS]"s"(rWS), [sP2]"s"(sP2), [sP3]"s"(sP3) : "memory"); }
  SB();
  b8 f1466 = cat(l1466, h1466);
  MA(dB10, f1466, H10_0); SB();
  s4 l1470 = TR(pB1, 40960);
  SB();
  const unsigned sT = 26214400u + ((Rb * 4 + j) * 4 + r) * 8192 + gi * 64, sTa = sT + cg * 4096, sTb = sTa ^ 4096;
  u2 ta, tb;
  { asm volatile("s_nop 2\n\tbuffer_load_dwordx2 %[ta], %[vT], %[rWS], %[sTa] offen sc1\n\tbuffer_load_dwordx2 %[tb], %[vT], %[rWS], %[sTb] offen offset:8 sc1" : [ta]"=&v"(ta), [tb]"=&v"(tb) : [vT]"v"(vT), [rWS]"s"(rWS), [sTa]"s"(sTa), [sTb]"s"(sTb) : "memory"); }
  SB();
  MA(dB11, f1466, H10_1); SB();
  s4 h1470 = TR(pB1, 41984);
  SB();
  b8 f1467 = cat(l1467, h1467);
  MA(dB00, f1467, H11_0); SB();
  s4 l1471 = TR(pB0, 45056);
  SB();
  MA(dB01, f1467, H11_1); SB();
  s4 h1471 = TR(pB0, 46080);
  SB();
  b8 f1468 = cat(l1468, h1468);
  MA(dB10, f1468, H11_0); SB();
  s4 l1472 = TR(pB1, 45056);
  SB();
  MA(dB11, f1468, H11_1); SB();
  s4 h1472 = TR(pB1, 46080);
  SB();
  b8 f1469 = cat(l1469, h1469);
  MA(dB00, f1469, H12_0); SB();
  s4 l1473 = TR(pB0, 49152);
  SB();
  const unsigned sFQ = sFP + (gi ^ 1) * 32;
  u2 pP1;
  { asm volatile("buffer_load_dwordx2 %[pP1], %[v1], %[rWS], %[sFQ] offen offset:128 sc1" : [pP1]"=&v"(pP1) : [v1]"v"(v1), [rWS]"s"(rWS), [sFQ]"s"(sFQ) : "memory"); }
  SB();
  MA(dB01, f1469, H12_1); SB();
  s4 h1473 = TR(pB0, 50176);
  SB();
  b8 f1470 = cat(l1470, h1470);
  MA(dB10, f1470, H12_0); SB();
  s4 l1474 = TR(pB1, 49152);
  SB();
  MA(dB11, f1470, H12_1); SB();
  s4 h1474 = TR(pB1, 50176);
  SB();
  b8 f1471 = cat(l1471, h1471);
  MA(dB00, f1471, H13_0); SB();
  s4 l1475 = TR(pB0, 53248);
  SB();
  MA(dB01, f1471, H13_1); SB();
  s4 h1475 = TR(pB0, 54272);
  SB();
  b8 f1472 = cat(l1472, h1472);
  MA(dB10, f1472, H13_0); SB();
  s4 l1476 = TR(pB1, 53248);
  SB();
  MA(dB11, f1472, H13_1); SB();
  s4 h1476 = TR(pB1, 54272);
  SB();
  b8 f1473 = cat(l1473, h1473);
  MA(dB00, f1473, H14_0); SB();
  s4 l1477 = TR(pB0, 0);
  SB();
  MA(dB01, f1473, H14_1); SB();
  s4 h1477 = TR(pB0, 1024);
  SB();
  b8 f1474 = cat(l1474, h1474);
  MA(dB10, f1474, H14_0); SB();
  s4 l1478 = TR(pB1, 0);
  SB();
  MA(dB11, f1474, H14_1); SB();
  s4 h1478 = TR(pB1, 1024);
  SB();
  b8 f1475 = cat(l1475, h1475);
  MA(dB00, f1475, H15_0); SB();
  s4 l1479 = TR(pB0, 4096);
  SB();
  MA(dB01, f1475, H15_1); SB();
  s4 h1479 = TR(pB0, 5120);
  SB();
  b8 f1476 = cat(l1476, h1476);
  MA(dB10, f1476, H15_0); SB();
  s4 l1480 = TR(pB1, 4096);
  SB();
  MA(dB11, f1476, H15_1); SB();
  s4 h1480 = TR(pB1, 5120);
  SB();
  b8 f1477 = cat(l1477, h1477);
  MA(dB00, f1477, H16_0); SB();
  pB0 += 4294893568u; OPQ(pB0);
  s4 l1481 = TR(pB0, 8192);
  SB();
  MA(dB01, f1477, H16_1); SB();
  s4 h1481 = TR(pB0, 9216);
  SB();
  b8 f1478 = cat(l1478, h1478);
  MA(dB10, f1478, H16_0); SB();
  pB1 += 4294893568u; OPQ(pB1);
  s4 l1482 = TR(pB1, 8192);
  SB();
  MA(dB11, f1478, H16_1); SB();
  s4 h1482 = TR(pB1, 9216);
  SB();
  b8 f1479 = cat(l1479, h1479);
  MA(dB00, f1479, H17_0); SB();
  s4 l1483 = TR(pB0, 12288);
  SB();
  MA(dB01, f1479, H17_1); SB();
  s4 h1483 = TR(pB0, 13312);
  SB();
  { asm volatile("s_waitcnt vmcnt(1)" : [q2]"+v"(q2), [q3]"+v"(q3), [ta]"+v"(ta), [tb]"+v"(tb) :  : "memory"); }
  SB();
  f4 ts; MZ(ts, asb(i2), asb(cat2(ta, tb))); SB();
  SB();
  MA(dB00, asb(i0), asb(q2)); SB();
  MA(dB00, asb(i0), asb(q3)); SB();
  SB();
  b8 f1480 = cat(l1480, h1480);
  MA(dB10, f1480, H17_0); SB();
  s4 l1484 = TR(pB1, 12288);
  SB();
  MA(dB11, f1480, H17_1); SB();
  s4 h1484 = TR(pB1, 13312);
  SB();
  SB();
  MA(dB10, asb(i1), asb(q2)); SB();
  MA(dB10, asb(i1), asb(q3)); SB();
  SB();
  b8 f1481 = cat(l1481, h1481);
  MA(dB00, f1481, H18_0); SB();
  s4 l1485 = TR(pB0, 60416);
  SB();
  { asm volatile("s_waitcnt vmcnt(0)" : [pP1]"+v"(pP1) :  : "memory"); }
  repoll<128>(pP1, rWS, v1, sFQ, T_); SB();
  SB();
  MA(dB01, f1481, H18_1); SB();
  const unsigned sP1 = 28311552u + ((Rb * 4 + j) * 4 + gi) * 16384 + w * 2048; u4 q0, q1;
  { asm volatile("s_nop 2\n\tbuffer_load_dwordx4 %[q0], %[v16], %[rWS], %[sP1] offen sc1\n\tbuffer_load_dwordx4 %[q1], %[v16], %[rWS], %[sP1] offen offset:1024 sc1" : [q0]"=&v"(q0), [q1]"=&v"(q1) : [v16]"v"(v16), [rWS]"s"(rWS), [sP1]"s"(sP1) : "memory"); }
  SB();
  SB();
  b8 f1482 = cat(l1482, h1482);
  MA(dB10, f1482, H18_0); SB();
  s4 l1486 = TR(pB1, 60416);
  SB();
  __builtin_amdgcn_raw_buffer_store_b64(pk4(ts), rO, vt, gi * 4608 + 1024 + 32 * j, 16);
  SB();
  MA(dB11, f1482, H18_1); SB();
  b8 f1483 = cat(l1483, h1483);
  MA(dB00, f1483, H19_0); SB();
  MA(dB01, f1483, H19_1); SB();
  b8 f1484 = cat(l1484, h1484);
  MA(dB10, f1484, H19_0); SB();
  MA(dB11, f1484, H19_1); SB();
  MH(dB00, l1485, HL0); SB();
  MH(dB01, l1485, HL1); SB();
  MH(dB10, l1486, HL0); SB();
  MH(dB11, l1486, HL1); SB();
  LR(ex1, 0) = __builtin_bit_cast(u4, dB01);
  LR(ex1, 1024) = __builtin_bit_cast(u4, dB11);
  SB();
  { asm volatile("s_waitcnt vmcnt(12) lgkmcnt(0)\n\ts_barrier" :  :  : "memory"); }
  SB();
  const f4 e3012_0 = __builtin_bit_cast(f4, LR(ex0, 0));
  const f4 e3012_1 = __builtin_bit_cast(f4, LR(ex0, 1024));
  SB();
  dB00 += e3012_0; dB10 += e3012_1;
  SB();
  { asm volatile("s_waitcnt vmcnt(1)" : [q0]"+v"(q0), [q1]"+v"(q1) :  : "memory"); }
  MF(dB00, asb(i0), asb(q0)); SB();
  MF(dB10, asb(i1), asb(q0)); SB();
  MF(dB00, asb(i0), asb(q1)); SB();
  MF(dB10, asb(i1), asb(q1)); SB();
  dB00 *= G0; dB10 *= G0;
  *(LDSP u2*)(wa + 2048) = pk4(dB00); SB(); *(LDSP u2*)(wa + 2080) = pk4(dB10); SB();
  const u4 ob_0 = LR(ra, 2048); SB();
  __builtin_amdgcn_raw_buffer_store_b128(ob_0, rO, vo, oB, 16); SB();
  st8((u2){tlo, thi}, rWS, vF, (b == 0 && w == 0) ? 0u : FAR);
  asm volatile("LC1_:");
}
extern "C" void kernel_launch(void* const* d_in, const int* in_sizes, int n_in, void* d_out, int out_size, void* d_ws, size_t ws_size,
                              hipStream_t stream) {
  gm_kernel<<<256, 512, 0, stream>>>((const char*)d_in[0], (const char*)d_in[1], (const char*)d_in[2], (const char*)d_in[4], (const char*)d_in[5],
                                     (const char*)d_in[6], (char*)d_out, (char*)d_ws);
}
```
